```python
import torch

def _softmax(x, axis=-1):
    return torch.softmax(x, dim=axis)
def _gelu(x):
    return torch.nn.functional.gelu(x, approximate="tanh")
def _silu(x):
    return torch.nn.functional.silu(x)
def _sigmoid(x):
    return torch.sigmoid(x)
def _layernorm(x, axis=-1, eps=1e-5):
    m = x.mean(dim=axis, keepdim=True)
    v = x.var(dim=axis, keepdim=True, unbiased=False)
    return (x - m) / torch.sqrt(v + eps)
def _scatter_add(tgt, idx, src, axis=0):
    return tgt.clone().index_add_(axis, idx, src)
def _segment_sum(src, seg, n):
    out = torch.zeros((n,) + src.shape[1:], dtype=src.dtype)
    return out.index_add_(0, seg, src)
def _pad(x, pw):
    flat = tuple(v for pair in reversed(pw) for v in pair)
    return torch.nn.functional.pad(x, flat)

kernel_name = 'gated_mix__mha_block__dense_activation_squared_relu__prompt'

def setup_inputs():

    _w = (lambda a: (lambda t: t.to(torch.bfloat16) if t.is_floating_point() else t)(torch.as_tensor(a)))
    def _A_setup():
        B, S, D, H = 1, 2048, 512, 32
        return {
            "x": _w((lambda *s: torch.randn(*s, dtype=torch.bfloat16) * 0.05)(B, S, D)),
            "wqkv": _w((lambda *s: torch.randn(*s, dtype=torch.bfloat16) * 0.8)(D, 3 * D)),
            "wo": _w((lambda *s: torch.randn(*s, dtype=torch.bfloat16) * 0.1)(D, D)),
        }
    _A_in = _A_setup()

    def _B_setup():
        B, S, D, F = 1, 2048, 512, 1792
        return {
            "x": _w((lambda *s: torch.randn(*s, dtype=torch.bfloat16) * 0.05)(B, S, D)),
            "w1": _w((lambda *s: torch.randn(*s, dtype=torch.bfloat16) * 0.1)(D, F)),
            "b1": _w((lambda *s: torch.randn(*s, dtype=torch.bfloat16) * 0.05)(F)),
            "w2": _w((lambda *s: torch.randn(*s, dtype=torch.bfloat16) * 0.05)(F, D)),
            "b2": _w((lambda *s: torch.randn(*s, dtype=torch.bfloat16) * 0.05)(D)),
        }
    _B_in = _B_setup()

    return {
        "A_x": _A_in["x"],
        "A_wqkv": _A_in["wqkv"],
        "A_wo": _A_in["wo"],
        "B_x": _B_in["x"],
        "B_w1": _B_in["w1"],
        "B_b1": _B_in["b1"],
        "B_w2": _B_in["w2"],
        "B_b2": _B_in["b2"],
        "gate_w": _w((lambda *s: torch.randn(*s, dtype=torch.bfloat16) * 0.05)(512) * 16.0),
    }

def reference(A_x, A_wqkv, A_wo, B_x, B_w1, B_b1, B_w2, B_b2, gate_w):
    def _A_ref(x, wqkv, wo):
        B, S, D = x.shape
        H = 32
        Dh = D // H
        qkv = (lambda x, *d: x.permute(*d))((x @ wqkv).reshape(B, S, 3, H, Dh), 2, 0, 3, 1, 4)
        q, k, v = qkv[0], qkv[1], qkv[2]
        scores = torch.einsum("bhqd,bhkd->bhqk", q, k) * (Dh ** -0.5)
        p = _softmax(scores, axis=-1)
        o = torch.einsum("bhqk,bhkd->bhqd", p, v)
        return (lambda x, *d: x.permute(*d))(o, 0, 2, 1, 3).reshape(B, S, D) @ wo

    def _B_ref(x, w1, b1, w2, b2):
        h = torch.einsum("bsd,df->bsf", x, w1) + b1
        zero = torch.zeros_like(h)
        r = torch.where(h > zero, h, zero)
        h = r * r
        out = torch.einsum("bsf,fd->bsd", h, w2) + b2
        return out

    x = A_x
    g = _sigmoid((x * gate_w).sum(-1, keepdims=True))
    a = _A_ref(x, A_wqkv, A_wo)
    b = _B_ref(x, B_w1, B_b1, B_w2, B_b2)
    return g * a + (1 - g) * b
```

```cpp
#include <hip/hip_runtime.h>
#include <hip/hip_bf16.h>
#include <stdint.h>

typedef __attribute__((ext_vector_type(8))) __bf16 bf16x8;
typedef __attribute__((ext_vector_type(2))) __bf16 bf16x2;
typedef __attribute__((ext_vector_type(4))) short s16x4;
typedef __attribute__((ext_vector_type(16))) float f32x16;
typedef __attribute__((ext_vector_type(4))) float f32x4;
typedef __attribute__((ext_vector_type(4))) unsigned u32x4;
typedef __attribute__((ext_vector_type(2))) unsigned u32x2;
#define LAS __attribute__((address_space(3)))
typedef LAS const char* lds_cptr;
typedef LAS char* lds_ptr;

constexpr int S = 2048, D = 512, NH = 32, FF = 1792;
constexpr int KSTEPS = 32 + FF / 16;

constexpr size_t WS_Q = 0;
constexpr size_t WS_KV = WS_Q + (size_t)NH * 64 * 1024;
constexpr size_t WS_A = WS_KV + (size_t)NH * 64 * 2048;
constexpr size_t WS_G = WS_A + (size_t)64 * KSTEPS * 1024;

#define MFMA(a, b, c) __builtin_amdgcn_mfma_f32_32x32x16_bf16(a, b, c, 0, 0, 0)
#define SBAR() __builtin_amdgcn_sched_barrier(0)

__device__ __forceinline__ unsigned cvtpk(float lo, float hi) {
  bf16x2 v; v[0] = (__bf16)lo; v[1] = (__bf16)hi; return __builtin_bit_cast(unsigned, v);
}
__device__ __forceinline__ float bfround(float x) { return (float)(__bf16)x; }
__device__ __forceinline__ float bf2f(unsigned short b) { return __uint_as_float((unsigned)b << 16); }
__device__ __forceinline__ s16x4 ldtr(lds_cptr p) {
  return __builtin_amdgcn_ds_read_tr16_b64_v4i16((LAS s16x4*)p);
}
__device__ __forceinline__ bf16x8 ldtr2(lds_cptr p0, lds_cptr p1) {
  s16x4 a = ldtr(p0), b = ldtr(p1);
  typedef __attribute__((ext_vector_type(8))) short s16x8;
  s16x8 r = {a[0], a[1], a[2], a[3], b[0], b[1], b[2], b[3]};
  return __builtin_bit_cast(bf16x8, r);
}
__device__ __forceinline__ bf16x8 ld128(lds_cptr p) { return *(const LAS bf16x8*)p; }
__device__ __forceinline__ void glds16(const char* sbase, unsigned voff, unsigned lds_base) {
  unsigned sv;
  asm volatile("s_mov_b32 %0, m0\n\ts_mov_b32 m0, %3\n\ts_nop 0\n\tglobal_load_lds_dwordx4 %1, %2\n\ts_mov_b32 m0, %0"
               : "=&s"(sv) : "v"(voff), "s"(sbase), "s"(lds_base) : "memory");
}
__device__ __forceinline__ float swap_add(float v) {
  auto rr = __builtin_amdgcn_permlane32_swap(__float_as_uint(v), __float_as_uint(v), false, false);
  return __uint_as_float(rr[0]) + __uint_as_float(rr[1]);
}
__device__ __forceinline__ float swap_max(float v) {
  auto rr = __builtin_amdgcn_permlane32_swap(__float_as_uint(v), __float_as_uint(v), false, false);
  return fmaxf(__uint_as_float(rr[0]), __uint_as_float(rr[1]));
}
__device__ __forceinline__ float dot8(bf16x8 a, bf16x8 b, float acc) {
#pragma unroll
  for (int i = 0; i < 4; ++i) {
    bf16x2 x = {a[2 * i], a[2 * i + 1]}, y = {b[2 * i], b[2 * i + 1]};
    acc = __builtin_amdgcn_fdot2_f32_bf16(x, y, acc, false);
  }
  return acc;
}
__device__ __forceinline__ float gate_from_logit(float z) {
  z = bfround(z);
  return bfround(1.0f / (1.0f + __expf(-z)));
}

__device__ __forceinline__ void spin_cycles(unsigned long long n) { unsigned long long t0 = __builtin_amdgcn_s_memtime(); while (__builtin_amdgcn_s_memtime() - t0 < n) __builtin_amdgcn_s_sleep(1); }
__global__ void empty_k(char* ws) { if (threadIdx.x == 1234567) ws[0] = 1; }
constexpr int ST_X = 0, ST_QK = 32768, ST_V = ST_QK + 4096, ST_W1 = ST_V + 2048, ST_BYTES = ST_W1 + 8192;
constexpr int K1_GATE = 3 * ST_BYTES, K1_B1 = K1_GATE + 1024, K1_LDS = K1_B1 + 1024;

__global__ __launch_bounds__(768, 3) void k1_proj(const char* __restrict__ x, const char* __restrict__ wqkv,
                                                  const char* __restrict__ w1, const char* __restrict__ b1,
                                                  const char* __restrict__ gw, char* __restrict__ ws) {
  extern __shared__ __attribute__((aligned(16))) char smem[];
  const int tid = threadIdx.x, lane = tid & 63;
  const int wid = __builtin_amdgcn_readfirstlane(tid >> 6);
  const int xg = blockIdx.x & 7, ig = blockIdx.x >> 3;
  const int qb = (xg & 1) * 4 + (ig & 3), h = (xg >> 1) * 8 + (ig >> 2);
  const int hf = h < 28 ? h : 27;
  const unsigned lds0 = (unsigned)(uintptr_t)smem;
  lds_cptr L = (lds_cptr)smem;

  const char* sb[4]; unsigned vo[4]; unsigned st[4]; unsigned lo[4]; unsigned rel[4];
#pragma unroll
  for (int i = 0; i < 4; ++i) {
    const int slot = wid * 4 + i;
    if (slot < 32) {
      const int row = slot * 8 + (lane >> 3);
      sb[i] = x + (size_t)qb * 256 * 1024; vo[i] = row * 1024 + (((lane & 7) ^ ((row >> 1) & 7)) << 4);
      st[i] = 128; lo[i] = ST_X + slot * 1024; rel[i] = 1;
    } else if (slot < 36) {
      const int k = (slot - 32) * 16 + (lane >> 2), p = lane & 3;
      sb[i] = wqkv + h * 32; vo[i] = k * 3072 + (p >> 1) * 1024 + (p & 1) * 16;
      st[i] = 64 * 3072; lo[i] = ST_QK + (slot - 32) * 1024; rel[i] = 1;
    } else if (slot < 38) {
      const int k = (slot - 36) * 32 + (lane >> 1);
      sb[i] = wqkv + 2048 + h * 32; vo[i] = k * 3072 + (lane & 1) * 16;
      st[i] = 64 * 3072; lo[i] = ST_V + (slot - 36) * 1024; rel[i] = 1;
    } else if (slot < 46) {
      const int k = (slot - 38) * 8 + (lane >> 3), p = lane & 7;
      sb[i] = w1 + hf * 128; vo[i] = k * (FF * 2) + ((p ^ (((k >> 1) & 1) << 2)) << 4);
      st[i] = 64 * FF * 2; lo[i] = ST_W1 + (slot - 38) * 1024; rel[i] = 1;
    } else if (slot == 46) {
      sb[i] = gw; vo[i] = lane * 16; st[i] = 0; lo[i] = K1_GATE; rel[i] = 0;
    } else {
      sb[i] = b1 + hf * 128; vo[i] = (lane & 7) * 16; st[i] = 0; lo[i] = K1_B1; rel[i] = 0;
    }
  }
#define ISSUE(c, slotbase)                                                                     \
  _Pragma("unroll") for (int i = 0; i < 4; ++i)                                                \
      glds16(sb[i] + (size_t)(c) * st[i], vo[i], lds0 + lo[i] + rel[i] * (slotbase));
  ISSUE(0, 0) ISSUE(1, ST_BYTES) ISSUE(2, 2 * ST_BYTES)

  const int r32 = lane & 31, hh = lane >> 5, g1 = (lane >> 4) & 1, q4 = (lane >> 2) & 3, p4 = lane & 3;
  f32x16 acc[4];
#pragma unroll
  for (int i = 0; i < 4; ++i) acc[i] = f32x16{};
  float ga0 = 0.f, ga1 = 0.f;
  const bool attn = wid < 8;
  const int row0 = attn ? wid * 32 + r32 : (wid - 8) * 64 + r32;
  const int sw = (row0 >> 1) & 7;
  const int xoff = row0 * 128;
  const int qkoff = ST_QK + (8 * hh + q4) * 64 + g1 * 32 + p4 * 8;
  const int voff = ST_V + (8 * hh + q4) * 32 + p4 * 8;
  const int w1off = ST_W1 + (8 * hh + q4) * 128 + g1 * 32 + p4 * 8;
  const int w1x = ((q4 >> 1) & 1) * 64;

#define CHUNK_SYNC(c)                                                                              \
  if (c == 0) asm volatile("s_waitcnt vmcnt(8) lgkmcnt(0)\n\ts_barrier" ::: "memory");                \
  else if (c < 7) asm volatile("s_waitcnt vmcnt(4) lgkmcnt(0)\n\ts_barrier" ::: "memory");            \
  else asm volatile("s_waitcnt vmcnt(0) lgkmcnt(0)\n\ts_barrier" ::: "memory");                       \
  if (c >= 1 && c + 2 < 8) { ISSUE(c + 2, ((c + 2) % 3) * ST_BYTES) }                                 \
  lds_cptr B = L + (c % 3) * ST_BYTES;
  if (attn) {
#pragma unroll
    for (int c = 0; c < 8; ++c) {
      CHUNK_SYNC(c)
#pragma unroll
      for (int s = 0; s < 4; ++s) {
        bf16x8 xf = ld128(B + xoff + (((2 * s + hh) ^ sw) << 4));
        bf16x8 wqk = ldtr2(B + qkoff + s * 1024, B + qkoff + s * 1024 + 256);
        bf16x8 wv = ldtr2(B + voff + s * 512, B + voff + s * 512 + 128);
        bf16x8 gf = ld128(L + K1_GATE + (c * 64 + s * 16 + 8 * hh) * 2);
        acc[0] = MFMA(wqk, xf, acc[0]);
        acc[1] = MFMA(xf, wv, acc[1]);
        ga0 = dot8(xf, gf, ga0);
      }
    }
  } else {
#pragma unroll
    for (int c = 0; c < 8; ++c) {
      CHUNK_SYNC(c)
#pragma unroll
      for (int s = 0; s < 4; ++s) {
        bf16x8 x0 = ld128(B + xoff + (((2 * s + hh) ^ sw) << 4));
        bf16x8 x1 = ld128(B + xoff + 4096 + (((2 * s + hh) ^ sw) << 4));
        bf16x8 wa = ldtr2(B + w1off + s * 2048 + w1x, B + w1off + s * 2048 + 512 + w1x);
        bf16x8 wb = ldtr2(B + w1off + s * 2048 + (w1x ^ 64), B + w1off + s * 2048 + 512 + (w1x ^ 64));
        bf16x8 gf = ld128(L + K1_GATE + (c * 64 + s * 16 + 8 * hh) * 2);
        acc[0] = MFMA(wa, x0, acc[0]);
        acc[1] = MFMA(wa, x1, acc[1]);
        acc[2] = MFMA(wb, x0, acc[2]);
        acc[3] = MFMA(wb, x1, acc[3]);
        ga0 = dot8(x0, gf, ga0);
        ga1 = dot8(x1, gf, ga1);
      }
    }
  }
  if (attn) {
    const int tile = qb * 8 + wid;
    const float qs = 0.25f * 1.4426950408889634f;
    u32x4 qv, kv;
#pragma unroll
    for (int i = 0; i < 4; ++i) {
      qv[i] = cvtpk(acc[0][2 * i] * qs, acc[0][2 * i + 1] * qs);
      kv[i] = cvtpk(acc[0][8 + 2 * i], acc[0][8 + 2 * i + 1]);
    }
    *(u32x4*)(ws + WS_Q + ((size_t)h * 64 + tile) * 1024 + lane * 16) = qv;
    char* kvb = ws + WS_KV + ((size_t)h * 64 + tile) * 2048;
    *(u32x4*)(kvb + lane * 16) = kv;
    u32x4 vv;
#pragma unroll
    for (int i = 0; i < 4; ++i) {
      const float a = g1 ? acc[1][8 + 2 * i] : acc[1][2 * i], b = g1 ? acc[1][8 + 2 * i + 1] : acc[1][2 * i + 1];
      vv[i] = cvtpk(a, b);
    }
    *(u32x4*)(kvb + 1024 + (2 * g1 + hh) * 256 + (lane & 15) * 16) = vv;
    if (h == 0) {
      const float g = gate_from_logit(swap_add(ga0));
      if (hh == 0) ((float*)(ws + WS_G))[tile * 32 + r32] = g;
    }
  } else if (h < 28) {
    const float og0 = bfround(1.0f - gate_from_logit(swap_add(ga0)));
    const float og1 = bfround(1.0f - gate_from_logit(swap_add(ga1)));
#pragma unroll
    for (int ft = 0; ft < 2; ++ft) {
      float bb[16];
#pragma unroll
      for (int rq = 0; rq < 4; ++rq) {
        u32x2 bw = *(const LAS u32x2*)(L + K1_B1 + (ft * 32 + 8 * rq + 4 * hh) * 2);
        bb[4 * rq] = __uint_as_float(bw[0] << 16); bb[4 * rq + 1] = __uint_as_float(bw[0] & 0xffff0000u);
        bb[4 * rq + 2] = __uint_as_float(bw[1] << 16); bb[4 * rq + 3] = __uint_as_float(bw[1] & 0xffff0000u);
      }
#pragma unroll
      for (int mt = 0; mt < 2; ++mt) {
        const float og = mt ? og1 : og0;
        const int tt = qb * 8 + (wid - 8) * 2 + mt;
#pragma unroll
        for (int s = 0; s < 2; ++s) {
          u32x4 hv;
#pragma unroll
          for (int i = 0; i < 4; ++i) {
            float a = fmaxf(acc[ft * 2 + mt][8 * s + 2 * i] + bb[8 * s + 2 * i], 0.f);
            float b = fmaxf(acc[ft * 2 + mt][8 * s + 2 * i + 1] + bb[8 * s + 2 * i + 1], 0.f);
            hv[i] = cvtpk(a * a * og, b * b * og);
          }
          *(u32x4*)(ws + WS_A + ((size_t)tt * KSTEPS + 32 + h * 4 + ft * 2 + s) * 1024 + lane * 16) = hv;
        }
      }
    }
  }
}

constexpr int TILE_B = 2112;
constexpr int K2_LDS = 64 * TILE_B;

__device__ __forceinline__ float max16(const f32x16& s) {
  float a = fmaxf(fmaxf(s[0], s[1]), s[2]);
#pragma unroll
  for (int r = 3; r < 15; r += 2) a = fmaxf(fmaxf(a, s[r]), s[r + 1]);
  return fmaxf(a, s[15]);
}
__device__ __forceinline__ void pack_p(const f32x16& s, bf16x8& p0, bf16x8& p1) {
  u32x4 a, b;
#pragma unroll
  for (int i = 0; i < 4; ++i) { a[i] = cvtpk(s[2 * i], s[2 * i + 1]); b[i] = cvtpk(s[8 + 2 * i], s[8 + 2 * i + 1]); }
  p0 = __builtin_bit_cast(bf16x8, a); p1 = __builtin_bit_cast(bf16x8, b);
}

__global__ __launch_bounds__(512, 2) void k2_attn(char* __restrict__ ws) {
  extern __shared__ __attribute__((aligned(16))) char smem[];
  const int tid = threadIdx.x, lane = tid & 63;
  const int wid = __builtin_amdgcn_readfirstlane(tid >> 6);
  const int qb = blockIdx.x >> 5, h = (blockIdx.x & 7) * 4 + ((blockIdx.x >> 3) & 3);
  const int r32 = lane & 31, hh = lane >> 5;
  lds_ptr L = (lds_ptr)smem;
  const char* kvg = ws + WS_KV + (size_t)h * 64 * 2048 + wid * 2048 + lane * 16;
  u32x4 kr[8], vr[8];
#pragma unroll
  for (int c = 0; c < 8; ++c) { kr[c] = *(const u32x4*)(kvg + c * 16384); vr[c] = *(const u32x4*)(kvg + c * 16384 + 1024); }
  const int tile = qb * 8 + wid;
  const bf16x8 qf = *(const bf16x8*)(ws + WS_Q + ((size_t)h * 64 + tile) * 1024 + lane * 16);
  const float g = ((const float*)(ws + WS_G))[tile * 32 + r32];
  if (tid < 256) *(LAS u32x4*)(L + (tid >> 2) * TILE_B + 1024 + (tid & 3) * 272 + 256) = u32x4{0x3f803f80u, 0x3f803f80u, 0x3f803f80u, 0x3f803f80u};
  lds_ptr kw = L + wid * TILE_B + lane * 16;
  lds_ptr vw = L + wid * TILE_B + 1024 + (lane >> 4) * 272 + (lane & 15) * 16;
#pragma unroll
  for (int c = 0; c < 2; ++c) { *(LAS u32x4*)(kw + c * 8 * TILE_B) = kr[c]; *(LAS u32x4*)(vw + c * 8 * TILE_B) = vr[c]; }
  __syncthreads();

  const int vrow = r32 < 16 ? r32 : ((r32 >= 20 && r32 < 28) ? 0 : 16);
  lds_cptr kp = (lds_cptr)L + lane * 16;
  lds_cptr vp = (lds_cptr)L + 1024 + hh * 272 + vrow * 16;
  f32x16 negm, o = f32x16{};
  {
    f32x16 s0 = MFMA(ld128(kp), qf, f32x16{});
    const float m = swap_max(max16(s0));
#pragma unroll
    for (int r = 0; r < 16; ++r) negm[r] = -m;
  }
  auto run_tiles = [&](int t0, int nt) {
    f32x16 s = MFMA(ld128(kp + t0 * TILE_B), qf, negm);
#pragma unroll 8
    for (int t = t0; t < t0 + nt; ++t) {
      const int tn = (t + 1 < t0 + nt) ? t + 1 : t;
      bf16x8 kf = ld128(kp + tn * TILE_B);
      bf16x8 v0 = ld128(vp + t * TILE_B), v1 = ld128(vp + t * TILE_B + 544);
      f32x16 sn = MFMA(kf, qf, negm);
#pragma unroll
      for (int r = 0; r < 16; ++r) s[r] = __builtin_amdgcn_exp2f(s[r]);
      bf16x8 p0, p1; pack_p(s, p0, p1);
      o = MFMA(v0, p0, o);
      o = MFMA(v1, p1, o);
      s = sn;
    }
  };
  run_tiles(0, 16);
#pragma unroll
  for (int c = 2; c < 8; ++c) { *(LAS u32x4*)(kw + c * 8 * TILE_B) = kr[c]; *(LAS u32x4*)(vw + c * 8 * TILE_B) = vr[c]; }
  __syncthreads();
  run_tiles(16, 48);

  float l = hh ? o[12] : o[8];
  bool ok = l < 1.2676506e30f;
#pragma unroll
  for (int r = 0; r < 8; ++r) ok = ok && (fabsf(o[r]) < 3.0e38f);
  if (__builtin_expect(!__all(ok), 0)) {
    float m = -INFINITY;
    o = f32x16{};
    for (int t = 0; t < 64; ++t) {
      f32x16 s = MFMA(ld128(kp + t * TILE_B), qf, f32x16{});
      const float mn = fmaxf(m, swap_max(max16(s)));
      const float al = __builtin_amdgcn_exp2f(m - mn);
      m = mn;
#pragma unroll
      for (int r = 0; r < 16; ++r) { s[r] = __builtin_amdgcn_exp2f(s[r] - m); o[r] *= al; }
      bf16x8 p0, p1; pack_p(s, p0, p1);
      o = MFMA(ld128(vp + t * TILE_B), p0, o);
      o = MFMA(ld128(vp + t * TILE_B + 544), p1, o);
    }
    l = hh ? o[12] : o[8];
  }
  const float sc = g / l;
  u32x4 ov;
#pragma unroll
  for (int i = 0; i < 4; ++i) ov[i] = cvtpk(o[2 * i] * sc, o[2 * i + 1] * sc);
  *(u32x4*)(ws + WS_A + ((size_t)tile * KSTEPS + h) * 1024 + lane * 16) = ov;
}

constexpr int P_ROW = 272, P_WAVE = 64 * P_ROW, K3_LDS = 8 * P_WAVE;

__global__ __launch_bounds__(512, 2) void k3_out(const char* __restrict__ wo, const char* __restrict__ w2,
                                                 const char* __restrict__ b2, const char* __restrict__ ws,
                                                 char* __restrict__ out) {
  extern __shared__ __attribute__((aligned(16))) char smem[];
  const int tid = threadIdx.x, lane = tid & 63;
  const int wid = __builtin_amdgcn_readfirstlane(tid >> 6);
  const int qb = blockIdx.x & 7, j = blockIdx.x >> 3;
  const int tt0 = qb * 8 + (j >> 3) * 2, ct = j & 7;
  const int r32 = lane & 31, hh = lane >> 5, g1 = (lane >> 4) & 1, q4 = (lane >> 2) & 3, p4 = lane & 3;
  lds_ptr Lw = (lds_ptr)smem + wid * P_WAVE;
  const char* ab = ws + WS_A + (size_t)tt0 * KSTEPS * 1024 + lane * 16;
  const int wl = (lane >> 3) * 1024 + (lane & 7) * 16 + ct * 128;
  constexpr int NS = 18, PD = 6;
  u32x4 wr[PD][2]; bf16x8 ar[PD][2];
  auto issue = [&](int i, int slot) {
    const int ks = i < 4 ? wid * 4 + i : 32 + wid * 14 + (i - 4);
    const char* wsrc = (i < 4 ? wo + (size_t)ks * 16 * 1024 : w2 + (size_t)(ks - 32) * 16 * 1024) + wl;
    wr[slot][0] = *(const u32x4*)wsrc; wr[slot][1] = *(const u32x4*)(wsrc + 8192);
    ar[slot][0] = *(const bf16x8*)(ab + (size_t)ks * 1024);
    ar[slot][1] = *(const bf16x8*)(ab + (size_t)(KSTEPS + ks) * 1024);
  };
#pragma unroll
  for (int i = 0; i < PD; ++i) issue(i, i);
  f32x16 acc[4];
#pragma unroll
  for (int i = 0; i < 4; ++i) acc[i] = f32x16{};
  const int wrow = lane >> 3;
  lds_ptr wdst = Lw + wrow * 128 + (((lane & 7) ^ (((wrow >> 1) & 1) << 2)) << 4);
  lds_cptr rsrc = (lds_cptr)Lw + (4 * hh + q4) * 128 + g1 * 32 + p4 * 8;
  const int wx = ((q4 >> 1) & 1) * 64;
#pragma unroll
  for (int i = 0; i < NS; ++i) {
    const int slot = i % PD, buf = (i & 1) * 2048;
    *(LAS u32x4*)(wdst + buf) = wr[slot][0];
    *(LAS u32x4*)(wdst + buf + 1024) = wr[slot][1];
    bf16x8 wa = ldtr2(rsrc + buf + wx, rsrc + buf + 1024 + wx);
    bf16x8 wb = ldtr2(rsrc + buf + (wx ^ 64), rsrc + buf + 1024 + (wx ^ 64));
    bf16x8 a0 = ar[slot][0], a1 = ar[slot][1];
    if (i + PD < NS) issue(i + PD, slot);
    acc[0] = MFMA(wa, a0, acc[0]);
    acc[1] = MFMA(wa, a1, acc[1]);
    acc[2] = MFMA(wb, a0, acc[2]);
    acc[3] = MFMA(wb, a1, acc[3]);
  }
#pragma unroll
  for (int c2 = 0; c2 < 2; ++c2)
#pragma unroll
    for (int mt = 0; mt < 2; ++mt)
#pragma unroll
      for (int rq = 0; rq < 4; ++rq) {
        f32x4 v = {acc[c2 * 2 + mt][4 * rq], acc[c2 * 2 + mt][4 * rq + 1], acc[c2 * 2 + mt][4 * rq + 2], acc[c2 * 2 + mt][4 * rq + 3]};
        *(LAS f32x4*)(Lw + (mt * 32 + r32) * P_ROW + (c2 * 32 + 8 * rq + 4 * hh) * 4) = v;
      }
  const int tok = tid >> 3, cg = tid & 7;
  const float og = bfround(1.0f - ((const float*)(ws + WS_G))[tt0 * 32 + tok]);
  const u32x4 bw = *(const u32x4*)(b2 + (ct * 64 + cg * 8) * 2);
  __syncthreads();
  f32x4 s0 = {0, 0, 0, 0}, s1 = {0, 0, 0, 0};
#pragma unroll
  for (int w = 0; w < 8; ++w) {
    lds_cptr p = (lds_cptr)smem + w * P_WAVE + tok * P_ROW + cg * 32;
    s0 += *(const LAS f32x4*)p; s1 += *(const LAS f32x4*)(p + 16);
  }
  u32x4 ov;
  ov[0] = cvtpk(s0[0] + og * __uint_as_float(bw[0] << 16), s0[1] + og * __uint_as_float(bw[0] & 0xffff0000u));
  ov[1] = cvtpk(s0[2] + og * __uint_as_float(bw[1] << 16), s0[3] + og * __uint_as_float(bw[1] & 0xffff0000u));
  ov[2] = cvtpk(s1[0] + og * __uint_as_float(bw[2] << 16), s1[1] + og * __uint_as_float(bw[2] & 0xffff0000u));
  ov[3] = cvtpk(s1[2] + og * __uint_as_float(bw[3] << 16), s1[3] + og * __uint_as_float(bw[3] & 0xffff0000u));
  *(u32x4*)(out + (size_t)(tt0 * 32 + tok) * 1024 + (ct * 64 + cg * 8) * 2) = ov;
}

extern "C" void kernel_launch(void* const* d_in, const int* in_sizes, int n_in, void* d_out, int out_size,
                              void* d_ws, size_t ws_size, hipStream_t stream) {
  const char* x = (const char*)d_in[0];
  const char* wqkv = (const char*)d_in[1];
  const char* wo = (const char*)d_in[2];
  const char* w1 = (const char*)d_in[4];
  const char* b1 = (const char*)d_in[5];
  const char* w2 = (const char*)d_in[6];
  const char* b2 = (const char*)d_in[7];
  const char* gw = (const char*)d_in[8];
  char* ws = (char*)d_ws;
  (void)hipFuncSetAttribute((const void*)k1_proj, hipFuncAttributeMaxDynamicSharedMemorySize, K1_LDS);
  (void)hipFuncSetAttribute((const void*)k2_attn, hipFuncAttributeMaxDynamicSharedMemorySize, K2_LDS);
  (void)hipFuncSetAttribute((const void*)k3_out, hipFuncAttributeMaxDynamicSharedMemorySize, K3_LDS);
  k1_proj<<<256, 768, K1_LDS, stream>>>(x, wqkv, w1, b1, gw, ws);
  k2_attn<<<256, 512, K2_LDS, stream>>>(ws);
  k3_out<<<256, 512, K3_LDS, stream>>>(wo, w2, b2, ws, (char*)d_out);
  empty_k<<<256, 256, 0, stream>>>(ws);
}
```

```cpp
#include <hip/hip_runtime.h>
#include <hip/hip_bf16.h>
#include <stdint.h>

typedef __attribute__((ext_vector_type(8))) __bf16 bf16x8;
typedef __attribute__((ext_vector_type(2))) __bf16 bf16x2;
typedef __attribute__((ext_vector_type(4))) short s16x4;
typedef __attribute__((ext_vector_type(8))) short s16x8;
typedef __attribute__((ext_vector_type(16))) float f32x16;
typedef __attribute__((ext_vector_type(4))) float f32x4;
typedef __attribute__((ext_vector_type(4))) unsigned u32x4;
typedef __attribute__((ext_vector_type(2))) unsigned u32x2;
typedef unsigned long long u64;
#define LAS __attribute__((address_space(3)))
#define GAS __attribute__((address_space(1)))
typedef LAS const char* lds_cptr;
typedef LAS char* lds_ptr;
typedef GAS u64 gu64;

constexpr int FF = 1792;
constexpr int KSTEPS = 32 + FF / 16;

constexpr unsigned WS_E = 0;
constexpr unsigned WS_FKV = 4096;
constexpr unsigned WS_FO = WS_FKV + 2048 * 64;
constexpr unsigned WS_FH = WS_FO + 2048 * 64;
constexpr unsigned WS_KV = 1u << 20;
constexpr unsigned WS_A = WS_KV + 32u * 64 * 2048;
constexpr unsigned WS_END = WS_A + 64u * KSTEPS * 1024;

constexpr int ST_X = 0, ST_QK = 32768, ST_V = ST_QK + 4096, ST_W1 = ST_V + 2048, ST_BYTES = ST_W1 + 8192;
constexpr int TILE_B = 2112;
constexpr int L_SCR = 64 * TILE_B;
constexpr int L_G4 = L_SCR + 16384;
constexpr int L_QX = L_G4 + 4096;
constexpr int L_G = L_QX + 4096, L_E = L_G + 1024, L_CNT = L_E + 16;
constexpr int L_JUNK = L_CNT + 48;
constexpr int LDS_BYTES = L_JUNK + 256;
constexpr int F_AUXP = 0;
constexpr int F_WO = 65536;
constexpr int P_ROW = 272, P_WAVE = 32 * P_ROW;

#define STAMP(i)
#define STAMP_DECL
#define STAMP_OUT
#define MFMA(a, b, c) __builtin_amdgcn_mfma_f32_32x32x16_bf16(a, b, c, 0, 0, 0)
#define SBAR() __builtin_amdgcn_sched_barrier(0)
#define PIN(x) asm volatile("" : "+v"(x))
#define RLX_AGENT __ATOMIC_RELAXED, __HIP_MEMORY_SCOPE_AGENT
#define SC1 16

__device__ __forceinline__ unsigned cvtpk(float lo, float hi) {
  bf16x2 v; v[0] = (__bf16)lo; v[1] = (__bf16)hi; return __builtin_bit_cast(unsigned, v);
}
__device__ __forceinline__ float bfround(float x) { return (float)(__bf16)x; }
__device__ __forceinline__ bf16x8 ldtr2(lds_cptr p0, lds_cptr p1) {
  s16x4 a = __builtin_amdgcn_ds_read_tr16_b64_v4i16((LAS s16x4*)p0), b = __builtin_amdgcn_ds_read_tr16_b64_v4i16((LAS s16x4*)p1);
  s16x8 r = {a[0], a[1], a[2], a[3], b[0], b[1], b[2], b[3]};
  return __builtin_bit_cast(bf16x8, r);
}
__device__ __forceinline__ bf16x8 ld128(lds_cptr p) { return *(const LAS bf16x8*)p; }
__device__ __forceinline__ void glds16(const char* sbase, unsigned voff, unsigned lds_base) {
  unsigned sv;
  asm volatile("s_mov_b32 %0, m0\n\ts_mov_b32 m0, %3\n\ts_nop 0\n\tglobal_load_lds_dwordx4 %1, %2\n\ts_mov_b32 m0, %0"
               : "=&s"(sv) : "v"(voff), "s"(sbase), "s"(__builtin_amdgcn_readfirstlane(lds_base)) : "memory");
}
__device__ __forceinline__ void touch_lines(const char* sbase, unsigned voff, unsigned lds_base) {
  unsigned sv;
  asm volatile("s_mov_b32 %0, m0\n\ts_mov_b32 m0, %3\n\ts_nop 0\n\tglobal_load_lds_dword %1, %2\n\ts_mov_b32 m0, %0"
               : "=&s"(sv) : "v"(voff), "s"(sbase), "s"(__builtin_amdgcn_readfirstlane(lds_base)) : "memory");
}
__device__ __forceinline__ void st_sc1(u32x4 v, __amdgpu_buffer_rsrc_t rs, unsigned off) { __builtin_amdgcn_raw_buffer_store_b128(v, rs, off, 0, SC1); }
__device__ __forceinline__ float swap_max(float v) {
  auto rr = __builtin_amdgcn_permlane32_swap(__float_as_uint(v), __float_as_uint(v), false, false);
  return fmaxf(__uint_as_float(rr[0]), __uint_as_float(rr[1]));
}
__device__ __forceinline__ float gate_from_logit(float z) {
  z = bfround(z);
  return bfround(1.0f / (1.0f + __expf(-z)));
}
__device__ __forceinline__ float max16(const f32x16& s) {
  float a = fmaxf(fmaxf(s[0], s[1]), s[2]);
#pragma unroll
  for (int r = 3; r < 15; r += 2) a = fmaxf(fmaxf(a, s[r]), s[r + 1]);
  return fmaxf(a, s[15]);
}
__device__ __forceinline__ void pack_p(const f32x16& s, bf16x8& p0, bf16x8& p1) {
  u32x4 a, b;
#pragma unroll
  for (int i = 0; i < 4; ++i) { a[i] = cvtpk(s[2 * i], s[2 * i + 1]); b[i] = cvtpk(s[8 + 2 * i], s[8 + 2 * i + 1]); }
  p0 = __builtin_bit_cast(bf16x8, a); p1 = __builtin_bit_cast(bf16x8, b);
}
__device__ __forceinline__ u64 make_tag(u64 e) {
  const unsigned lo = __builtin_amdgcn_readfirstlane((unsigned)e), hi = __builtin_amdgcn_readfirstlane((unsigned)(e >> 32));
  return (((u64)hi << 32) | lo) * 0x9E3779B97F4A7C15ull + 0xD1B54A32D192ED03ull;
}
__device__ __forceinline__ void lds_arrive(unsigned addr, unsigned n, int lane) {
  asm volatile("s_waitcnt lgkmcnt(0)" ::: "memory");
  if (lane == 0) asm volatile("ds_add_u32 %0, %1" :: "v"(addr), "v"(n) : "memory");
}
constexpr unsigned SPIN_CAP = 1u << 20;
__device__ __forceinline__ void lds_wait(lds_cptr p, unsigned want) {
#pragma unroll 1
  for (unsigned spins = 0; spins < SPIN_CAP; ++spins) {
    const unsigned v = __builtin_amdgcn_readfirstlane(*(const volatile LAS unsigned*)p);
    if (v >= want) break;
    __builtin_amdgcn_s_sleep(1);
  }
  asm volatile("" ::: "memory");
}
__device__ __forceinline__ void flags_wait(const char* ws, unsigned off, u64 tag) {
  gu64* f = (gu64*)(ws + off);
#pragma unroll 1
  for (unsigned spins = 0; spins < (SPIN_CAP >> 4); ++spins) {
    const bool ok = __hip_atomic_load(f, RLX_AGENT) == tag;
    if (__all(ok)) break;
    __builtin_amdgcn_s_sleep(2);
  }
  asm volatile("" ::: "memory");
}
__device__ __forceinline__ void flag_set(char* ws, unsigned off, u64 tag, int lane) {
  asm volatile("s_waitcnt vmcnt(0)" ::: "memory");
  if (lane == 0) __hip_atomic_store((gu64*)(ws + off), tag, RLX_AGENT);
}

#define ATT_ASM \
  "s_nop 15\n" \
  "Latt_loop%=:\n" \
  "s_waitcnt lgkmcnt(2)\n" \
  "v_mfma_f32_32x32x16_bf16 v[80:95], v[132:135], v[128:131], v[96:111]\n" \
  "ds_read_b128 v[132:135], v152 offset:4224\n" \
  "v_exp_f32 v64, v64\n" \
  "v_exp_f32 v65, v65\n" \
  "v_exp_f32 v66, v66\n" \
  "v_exp_f32 v67, v67\n" \
  "s_waitcnt lgkmcnt(2)\n" \
  "v_mfma_f32_32x32x16_bf16 v[112:127], v[140:143], v[148:151], v[112:127]\n" \
  "ds_read_b128 v[140:143], v153 offset:544\n" \
  "v_exp_f32 v68, v68\n" \
  "v_exp_f32 v69, v69\n" \
  "v_exp_f32 v70, v70\n" \
  "v_exp_f32 v71, v71\n" \
  "v_cvt_pk_bf16_f32 v144, v64, v65\n" \
  "v_cvt_pk_bf16_f32 v145, v66, v67\n" \
  "v_cvt_pk_bf16_f32 v146, v68, v69\n" \
  "v_cvt_pk_bf16_f32 v147, v70, v71\n" \
  "v_exp_f32 v72, v72\n" \
  "v_exp_f32 v73, v73\n" \
  "v_exp_f32 v74, v74\n" \
  "v_exp_f32 v75, v75\n" \
  "s_waitcnt lgkmcnt(2)\n" \
  "v_mfma_f32_32x32x16_bf16 v[112:127], v[136:139], v[144:147], v[112:127]\n" \
  "ds_read_b128 v[136:139], v153 offset:2112\n" \
  "v_exp_f32 v76, v76\n" \
  "v_exp_f32 v77, v77\n" \
  "v_exp_f32 v78, v78\n" \
  "v_exp_f32 v79, v79\n" \
  "v_cvt_pk_bf16_f32 v148, v72, v73\n" \
  "v_cvt_pk_bf16_f32 v149, v74, v75\n" \
  "v_cvt_pk_bf16_f32 v150, v76, v77\n" \
  "v_cvt_pk_bf16_f32 v151, v78, v79\n" \
  "s_waitcnt lgkmcnt(2)\n" \
  "v_mfma_f32_32x32x16_bf16 v[64:79], v[132:135], v[128:131], v[96:111]\n" \
  "ds_read_b128 v[132:135], v152 offset:6336\n" \
  "v_exp_f32 v80, v80\n" \
  "v_exp_f32 v81, v81\n" \
  "v_exp_f32 v82, v82\n" \
  "v_exp_f32 v83, v83\n" \
  "s_waitcnt lgkmcnt(2)\n" \
  "v_mfma_f32_32x32x16_bf16 v[112:127], v[140:143], v[148:151], v[112:127]\n" \
  "ds_read_b128 v[140:143], v153 offset:2656\n" \
  "v_exp_f32 v84, v84\n" \
  "v_exp_f32 v85, v85\n" \
  "v_exp_f32 v86, v86\n" \
  "v_exp_f32 v87, v87\n" \
  "v_cvt_pk_bf16_f32 v144, v80, v81\n" \
  "v_cvt_pk_bf16_f32 v145, v82, v83\n" \
  "v_cvt_pk_bf16_f32 v146, v84, v85\n" \
  "v_cvt_pk_bf16_f32 v147, v86, v87\n" \
  "v_exp_f32 v88, v88\n" \
  "v_exp_f32 v89, v89\n" \
  "v_exp_f32 v90, v90\n" \
  "v_exp_f32 v91, v91\n" \
  "s_waitcnt lgkmcnt(2)\n" \
  "v_mfma_f32_32x32x16_bf16 v[112:127], v[136:139], v[144:147], v[112:127]\n" \
  "ds_read_b128 v[136:139], v153 offset:4224\n" \
  "v_exp_f32 v92, v92\n" \
  "v_exp_f32 v93, v93\n" \
  "v_exp_f32 v94, v94\n" \
  "v_exp_f32 v95, v95\n" \
  "v_cvt_pk_bf16_f32 v148, v88, v89\n" \
  "v_cvt_pk_bf16_f32 v149, v90, v91\n" \
  "v_cvt_pk_bf16_f32 v150, v92, v93\n" \
  "v_cvt_pk_bf16_f32 v151, v94, v95\n" \
  "s_waitcnt lgkmcnt(2)\n" \
  "v_mfma_f32_32x32x16_bf16 v[80:95], v[132:135], v[128:131], v[96:111]\n" \
  "ds_read_b128 v[132:135], v152 offset:8448\n" \
  "v_exp_f32 v64, v64\n" \
  "v_exp_f32 v65, v65\n" \
  "v_exp_f32 v66, v66\n" \
  "v_exp_f32 v67, v67\n" \
  "s_waitcnt lgkmcnt(2)\n" \
  "v_mfma_f32_32x32x16_bf16 v[112:127], v[140:143], v[148:151], v[112:127]\n" \
  "ds_read_b128 v[140:143], v153 offset:4768\n" \
  "v_exp_f32 v68, v68\n" \
  "v_exp_f32 v69, v69\n" \
  "v_exp_f32 v70, v70\n" \
  "v_exp_f32 v71, v71\n" \
  "v_cvt_pk_bf16_f32 v144, v64, v65\n" \
  "v_cvt_pk_bf16_f32 v145, v66, v67\n" \
  "v_cvt_pk_bf16_f32 v146, v68, v69\n" \
  "v_cvt_pk_bf16_f32 v147, v70, v71\n" \
  "v_exp_f32 v72, v72\n" \
  "v_exp_f32 v73, v73\n" \
  "v_exp_f32 v74, v74\n" \
  "v_exp_f32 v75, v75\n" \
  "s_waitcnt lgkmcnt(2)\n" \
  "v_mfma_f32_32x32x16_bf16 v[112:127], v[136:139], v[144:147], v[112:127]\n" \
  "ds_read_b128 v[136:139], v153 offset:6336\n" \
  "v_exp_f32 v76, v76\n" \
  "v_exp_f32 v77, v77\n" \
  "v_exp_f32 v78, v78\n" \
  "v_exp_f32 v79, v79\n" \
  "v_cvt_pk_bf16_f32 v148, v72, v73\n" \
  "v_cvt_pk_bf16_f32 v149, v74, v75\n" \
  "v_cvt_pk_bf16_f32 v150, v76, v77\n" \
  "v_cvt_pk_bf16_f32 v151, v78, v79\n" \
  "s_waitcnt lgkmcnt(2)\n" \
  "v_mfma_f32_32x32x16_bf16 v[64:79], v[132:135], v[128:131], v[96:111]\n" \
  "ds_read_b128 v[132:135], v152 offset:10560\n" \
  "v_exp_f32 v80, v80\n" \
  "v_exp_f32 v81, v81\n" \
  "v_exp_f32 v82, v82\n" \
  "v_exp_f32 v83, v83\n" \
  "s_waitcnt lgkmcnt(2)\n" \
  "v_mfma_f32_32x32x16_bf16 v[112:127], v[140:143], v[148:151], v[112:127]\n" \
  "ds_read_b128 v[140:143], v153 offset:6880\n" \
  "v_exp_f32 v84, v84\n" \
  "v_exp_f32 v85, v85\n" \
  "v_exp_f32 v86, v86\n" \
  "v_exp_f32 v87, v87\n" \
  "v_cvt_pk_bf16_f32 v144, v80, v81\n" \
  "v_cvt_pk_bf16_f32 v145, v82, v83\n" \
  "v_cvt_pk_bf16_f32 v146, v84, v85\n" \
  "v_cvt_pk_bf16_f32 v147, v86, v87\n" \
  "v_exp_f32 v88, v88\n" \
  "v_exp_f32 v89, v89\n" \
  "v_exp_f32 v90, v90\n" \
  "v_exp_f32 v91, v91\n" \
  "s_waitcnt lgkmcnt(2)\n" \
  "v_mfma_f32_32x32x16_bf16 v[112:127], v[136:139], v[144:147], v[112:127]\n" \
  "ds_read_b128 v[136:139], v153 offset:8448\n" \
  "v_exp_f32 v92, v92\n" \
  "v_exp_f32 v93, v93\n" \
  "v_exp_f32 v94, v94\n" \
  "v_exp_f32 v95, v95\n" \
  "v_cvt_pk_bf16_f32 v148, v88, v89\n" \
  "v_cvt_pk_bf16_f32 v149, v90, v91\n" \
  "v_cvt_pk_bf16_f32 v150, v92, v93\n" \
  "v_cvt_pk_bf16_f32 v151, v94, v95\n" \
  "s_waitcnt lgkmcnt(2)\n" \
  "v_mfma_f32_32x32x16_bf16 v[80:95], v[132:135], v[128:131], v[96:111]\n" \
  "ds_read_b128 v[132:135], v152 offset:12672\n" \
  "v_exp_f32 v64, v64\n" \
  "v_exp_f32 v65, v65\n" \
  "v_exp_f32 v66, v66\n" \
  "v_exp_f32 v67, v67\n" \
  "s_waitcnt lgkmcnt(2)\n" \
  "v_mfma_f32_32x32x16_bf16 v[112:127], v[140:143], v[148:151], v[112:127]\n" \
  "ds_read_b128 v[140:143], v153 offset:8992\n" \
  "v_exp_f32 v68, v68\n" \
  "v_exp_f32 v69, v69\n" \
  "v_exp_f32 v70, v70\n" \
  "v_exp_f32 v71, v71\n" \
  "v_cvt_pk_bf16_f32 v144, v64, v65\n" \
  "v_cvt_pk_bf16_f32 v145, v66, v67\n" \
  "v_cvt_pk_bf16_f32 v146, v68, v69\n" \
  "v_cvt_pk_bf16_f32 v147, v70, v71\n" \
  "v_exp_f32 v72, v72\n" \
  "v_exp_f32 v73, v73\n" \
  "v_exp_f32 v74, v74\n" \
  "v_exp_f32 v75, v75\n" \
  "s_waitcnt lgkmcnt(2)\n" \
  "v_mfma_f32_32x32x16_bf16 v[112:127], v[136:139], v[144:147], v[112:127]\n" \
  "ds_read_b128 v[136:139], v153 offset:10560\n" \
  "ds_read_b32 v155, v154\n" \
  "v_exp_f32 v76, v76\n" \
  "v_exp_f32 v77, v77\n" \
  "v_exp_f32 v78, v78\n" \
  "v_exp_f32 v79, v79\n" \
  "v_cvt_pk_bf16_f32 v148, v72, v73\n" \
  "v_cvt_pk_bf16_f32 v149, v74, v75\n" \
  "v_cvt_pk_bf16_f32 v150, v76, v77\n" \
  "v_cvt_pk_bf16_f32 v151, v78, v79\n" \
  "s_waitcnt lgkmcnt(2)\n" \
  "v_mfma_f32_32x32x16_bf16 v[64:79], v[132:135], v[128:131], v[96:111]\n" \
  "ds_read_b128 v[132:135], v152 offset:14784\n" \
  "v_exp_f32 v80, v80\n" \
  "v_exp_f32 v81, v81\n" \
  "v_exp_f32 v82, v82\n" \
  "v_exp_f32 v83, v83\n" \
  "s_waitcnt lgkmcnt(2)\n" \
  "v_mfma_f32_32x32x16_bf16 v[112:127], v[140:143], v[148:151], v[112:127]\n" \
  "ds_read_b128 v[140:143], v153 offset:11104\n" \
  "v_exp_f32 v84, v84\n" \
  "v_exp_f32 v85, v85\n" \
  "v_exp_f32 v86, v86\n" \
  "v_exp_f32 v87, v87\n" \
  "v_cvt_pk_bf16_f32 v144, v80, v81\n" \
  "v_cvt_pk_bf16_f32 v145, v82, v83\n" \
  "v_cvt_pk_bf16_f32 v146, v84, v85\n" \
  "v_cvt_pk_bf16_f32 v147, v86, v87\n" \
  "v_exp_f32 v88, v88\n" \
  "v_exp_f32 v89, v89\n" \
  "v_exp_f32 v90, v90\n" \
  "v_exp_f32 v91, v91\n" \
  "s_waitcnt lgkmcnt(2)\n" \
  "v_mfma_f32_32x32x16_bf16 v[112:127], v[136:139], v[144:147], v[112:127]\n" \
  "ds_read_b128 v[136:139], v153 offset:12672\n" \
  "v_exp_f32 v92, v92\n" \
  "v_exp_f32 v93, v93\n" \
  "v_exp_f32 v94, v94\n" \
  "v_exp_f32 v95, v95\n" \
  "v_cvt_pk_bf16_f32 v148, v88, v89\n" \
  "v_cvt_pk_bf16_f32 v149, v90, v91\n" \
  "v_cvt_pk_bf16_f32 v150, v92, v93\n" \
  "v_cvt_pk_bf16_f32 v151, v94, v95\n" \
  "v_readfirstlane_b32 %[tmp], v155\n" \
  "s_cmp_ge_u32 %[tmp], 8\n" \
  "s_cbranch_scc1 Latt_ok%=\n" \
  "s_mov_b32 %[spin], 0x100000\n" \
  "Latt_spin%=:\n" \
  "s_sleep 1\n" \
  "ds_read_b32 v155, v154\n" \
  "s_waitcnt lgkmcnt(0)\n" \
  "v_readfirstlane_b32 %[tmp], v155\n" \
  "s_sub_u32 %[spin], %[spin], 1\n" \
  "s_cmp_eq_u32 %[spin], 0\n" \
  "s_cbranch_scc1 Latt_ok%=\n" \
  "s_cmp_lt_u32 %[tmp], 8\n" \
  "s_cbranch_scc1 Latt_spin%=\n" \
  "Latt_ok%=:\n" \
  "s_waitcnt lgkmcnt(2)\n" \
  "v_mfma_f32_32x32x16_bf16 v[80:95], v[132:135], v[128:131], v[96:111]\n" \
  "ds_read_b128 v[132:135], v152 offset:16896\n" \
  "v_exp_f32 v64, v64\n" \
  "v_exp_f32 v65, v65\n" \
  "v_exp_f32 v66, v66\n" \
  "v_exp_f32 v67, v67\n" \
  "s_waitcnt lgkmcnt(2)\n" \
  "v_mfma_f32_32x32x16_bf16 v[112:127], v[140:143], v[148:151], v[112:127]\n" \
  "ds_read_b128 v[140:143], v153 offset:13216\n" \
  "v_exp_f32 v68, v68\n" \
  "v_exp_f32 v69, v69\n" \
  "v_exp_f32 v70, v70\n" \
  "v_exp_f32 v71, v71\n" \
  "v_cvt_pk_bf16_f32 v144, v64, v65\n" \
  "v_cvt_pk_bf16_f32 v145, v66, v67\n" \
  "v_cvt_pk_bf16_f32 v146, v68, v69\n" \
  "v_cvt_pk_bf16_f32 v147, v70, v71\n" \
  "v_exp_f32 v72, v72\n" \
  "v_exp_f32 v73, v73\n" \
  "v_exp_f32 v74, v74\n" \
  "v_exp_f32 v75, v75\n" \
  "s_waitcnt lgkmcnt(2)\n" \
  "v_mfma_f32_32x32x16_bf16 v[112:127], v[136:139], v[144:147], v[112:127]\n" \
  "ds_read_b128 v[136:139], v153 offset:14784\n" \
  "v_exp_f32 v76, v76\n" \
  "v_exp_f32 v77, v77\n" \
  "v_exp_f32 v78, v78\n" \
  "v_exp_f32 v79, v79\n" \
  "v_cvt_pk_bf16_f32 v148, v72, v73\n" \
  "v_cvt_pk_bf16_f32 v149, v74, v75\n" \
  "v_cvt_pk_bf16_f32 v150, v76, v77\n" \
  "v_cvt_pk_bf16_f32 v151, v78, v79\n" \
  "s_waitcnt lgkmcnt(2)\n" \
  "v_mfma_f32_32x32x16_bf16 v[64:79], v[132:135], v[128:131], v[96:111]\n" \
  "ds_read_b128 v[132:135], v152 offset:19008\n" \
  "v_exp_f32 v80, v80\n" \
  "v_exp_f32 v81, v81\n" \
  "v_exp_f32 v82, v82\n" \
  "v_exp_f32 v83, v83\n" \
  "s_waitcnt lgkmcnt(2)\n" \
  "v_mfma_f32_32x32x16_bf16 v[112:127], v[140:143], v[148:151], v[112:127]\n" \
  "ds_read_b128 v[140:143], v153 offset:15328\n" \
  "v_exp_f32 v84, v84\n" \
  "v_exp_f32 v85, v85\n" \
  "v_exp_f32 v86, v86\n" \
  "v_exp_f32 v87, v87\n" \
  "v_cvt_pk_bf16_f32 v144, v80, v81\n" \
  "v_cvt_pk_bf16_f32 v145, v82, v83\n" \
  "v_cvt_pk_bf16_f32 v146, v84, v85\n" \
  "v_cvt_pk_bf16_f32 v147, v86, v87\n" \
  "v_exp_f32 v88, v88\n" \
  "v_exp_f32 v89, v89\n" \
  "v_exp_f32 v90, v90\n" \
  "v_exp_f32 v91, v91\n" \
  "s_waitcnt lgkmcnt(2)\n" \
  "v_mfma_f32_32x32x16_bf16 v[112:127], v[136:139], v[144:147], v[112:127]\n" \
  "ds_read_b128 v[136:139], v153 offset:16896\n" \
  "v_exp_f32 v92, v92\n" \
  "v_exp_f32 v93, v93\n" \
  "v_exp_f32 v94, v94\n" \
  "v_exp_f32 v95, v95\n" \
  "v_cvt_pk_bf16_f32 v148, v88, v89\n" \
  "v_cvt_pk_bf16_f32 v149, v90, v91\n" \
  "v_cvt_pk_bf16_f32 v150, v92, v93\n" \
  "v_cvt_pk_bf16_f32 v151, v94, v95\n" \
  "v_add_u32 v152, 16896, v152\n" \
  "v_add_u32 v153, 16896, v153\n" \
  "v_add_u32 v154, 4, v154\n" \
  "s_sub_u32 %[n], %[n], 1\n" \
  "s_cmp_lg_u32 %[n], 0\n" \
  "s_cbranch_scc1 Latt_loop%=\n" \
  "s_waitcnt lgkmcnt(0)\n" \
  "s_nop 7\n"

__global__ __launch_bounds__(768, 3) void fused_k(const char* __restrict__ x, const char* __restrict__ wqkv,
                                                  const char* __restrict__ wo, const char* __restrict__ w1,
                                                  const char* __restrict__ b1, const char* __restrict__ w2,
                                                  const char* __restrict__ b2, const char* __restrict__ gw,
                                                  char* ws, char* __restrict__ out) {
  extern __shared__ __attribute__((aligned(16))) char smem[];
  const int tid = threadIdx.x, lane = tid & 63;
  const int wid = __builtin_amdgcn_readfirstlane(tid >> 6);
  const int xg = blockIdx.x & 7, ig = blockIdx.x >> 3;
  const int qb = (xg & 1) * 4 + (ig & 3), h = (xg >> 1) * 8 + (ig >> 2);
  const int hf = h < 28 ? h : 27;
  const unsigned lds0 = __builtin_amdgcn_readfirstlane((unsigned)(uintptr_t)smem);
  lds_ptr L = (lds_ptr)smem;
  const __amdgpu_buffer_rsrc_t rs = __builtin_amdgcn_make_buffer_rsrc(ws, 0, WS_END, 0x00020000);
  const int r32 = lane & 31, hh = lane >> 5, g1 = (lane >> 4) & 1, q4 = (lane >> 2) & 3, p4 = lane & 3;
  const int u = h >> 3, ct = h & 7;
  const lds_cptr cntp = (lds_cptr)L + L_CNT;
  STAMP_DECL
  STAMP(0);
#define BAR() asm volatile("s_waitcnt lgkmcnt(0)\n\ts_barrier" ::: "memory")

  if (wid < 8) {
    bf16x8 qf;
    int tile;
    if (wid >= 4) {
      const int j = wid - 4;
      const char* xs = x + (size_t)(qb * 256 + j * 64) * 1024;
      const unsigned xv0 = (lane >> 3) * 1024 + (((lane & 7) ^ (lane >> 4)) << 4), xv1 = (lane >> 3) * 1024 + (((lane & 7) ^ ((lane >> 4) + 4)) << 4);
      const char* qs_ = wqkv + h * 32 + (size_t)j * 16 * 3072;
      const unsigned qv_ = (lane >> 2) * 3072 + ((lane >> 1) & 1) * 1024 + (lane & 1) * 16;
      const unsigned vv_ = ((lane & 31) >> 1) * 3072 + 2048 + (lane & 1) * 16;
      const char* w1s = w1 + hf * 128 + (size_t)j * 16 * (FF * 2);
      const unsigned w1v = (lane >> 3) * (FF * 2) + (((lane & 7) ^ (((lane >> 4) & 1) << 2)) << 4);
#define ISSUE(c) { const unsigned sb_ = lds0 + ((c) % 3) * ST_BYTES;                                          \
        glds16(qs_ + (size_t)(c) * 64 * 3072, qv_, sb_ + ST_QK + j * 1024);                                   \
        if (lane < 32) glds16(qs_ + (size_t)(c) * 64 * 3072, vv_, sb_ + ST_V + j * 512);                      \
        _Pragma("unroll") for (int i = 0; i < 8; ++i)                                                         \
          glds16(xs + (c) * 128 + i * 8192, (i & 1) ? xv1 : xv0, sb_ + ST_X + (j * 8 + i) * 1024);            \
        glds16(w1s + (size_t)(c) * 64 * (FF * 2), w1v, sb_ + ST_W1 + j * 2048);                               \
        glds16(w1s + (size_t)((c) * 64 + 8) * (FF * 2), w1v, sb_ + ST_W1 + j * 2048 + 1024); }
      ISSUE(0) ISSUE(1) ISSUE(2)
      STAMP(18);
#pragma unroll
      for (int c = 0; c < 8; ++c) {
        if (c == 0) asm volatile("s_waitcnt vmcnt(24)\n\ts_barrier" ::: "memory");
        else if (c < 7) asm volatile("s_waitcnt vmcnt(12)\n\ts_barrier" ::: "memory");
        else asm volatile("s_waitcnt vmcnt(0)\n\ts_barrier" ::: "memory");
        STAMP(10 + c);
        if (c >= 1 && c + 2 < 8) ISSUE(c + 2)
      }
      STAMP(1);
      tile = qb * 8 + 2 * j + 1;
      lds_wait(cntp, 8);
      qf = ld128((lds_cptr)L + L_QX + j * 1024 + lane * 16);
    } else {
      if (wid == 2) {
        const char* xw = x + (size_t)((xg & 1) * 1024 + ig * 32) * 1024 + 384;
        touch_lines(xw, r32 * 1024 + hh * 128, lds0 + L_JUNK);
        touch_lines(xw, r32 * 1024 + 256 + hh * 128, lds0 + L_JUNK);
        touch_lines(xw, r32 * 1024 + 512, lds0 + L_JUNK);
      } else if (wid == 3) {
        const int r10 = lane < 60 ? lane / 6 : 0, c6 = lane % 6, ln = (xg >> 1) * 8 + (lane & 7);
        touch_lines(wqkv + (size_t)(192 + ig * 10) * 3072 + (xg >> 1) * 256, r10 * 3072 + (c6 >> 1) * 1024 + (c6 & 1) * 128, lds0 + L_JUNK);
        const char* ww = w1 + (size_t)(192 + ig * 10) * (FF * 2);
        touch_lines(ww, (lane >> 3) * (FF * 2) + (ln < 28 ? ln : 27) * 128, lds0 + L_JUNK);
        touch_lines(ww, (8 + ((lane >> 3) & 1)) * (FF * 2) + (ln < 28 ? ln : 27) * 128, lds0 + L_JUNK);
      } else if (wid == 0) {
        const u32x4 gv = *(const u32x4*)(gw + lane * 16);
#pragma unroll
        for (int i = 0; i < 4; ++i) {
          *(LAS unsigned short*)(L + L_G4 + lane * 64 + i * 16) = (unsigned short)gv[i];
          *(LAS unsigned short*)(L + L_G4 + lane * 64 + i * 16 + 8) = (unsigned short)(gv[i] >> 16);
        }
      } else if (wid == 1) {
        if (lane == 0) *(LAS u64*)(L + L_E) = __hip_atomic_load((gu64*)(ws + WS_E), RLX_AGENT);
        if (lane < 12) *(LAS unsigned*)(L + L_CNT + lane * 4) = 0u;
      }
      const int row0 = wid * 64 + r32, sw = (row0 >> 1) & 7;
      const lds_cptr xb = (lds_cptr)L + row0 * 128;
      const lds_cptr qkb = (lds_cptr)L + ST_QK + (8 * hh + q4) * 64 + g1 * 32 + p4 * 8;
      unsigned vb0 = lds0 + (g1 ? L_G4 + (8 * hh + q4) * 8 : ST_V + (8 * hh + q4) * 32 + p4 * 8), vb1 = vb0 + (g1 ? 32 : 128);
      unsigned vstep = g1 ? 128 : 512, vfwd = g1 ? 128 : ST_BYTES - 1536, vback = g1 ? 128 : -2 * ST_BYTES - 1536;
      PIN(vstep); PIN(vfwd); PIN(vback);
      f32x16 aqk0 = f32x16{}, aqk1 = f32x16{}, av0 = f32x16{}, av1 = f32x16{};
      bf16x8 fx0[2], fx1[2], fqk[2], fv[2];
#define LOAD_A(t, b) { const int so_ = (((t) >> 2) % 3) * ST_BYTES, s_ = (t) & 3;                              \
      fx0[b] = ld128(xb + so_ + (((2 * s_ + hh) ^ sw) << 4));                                                  \
      fqk[b] = ldtr2(qkb + so_ + s_ * 1024, qkb + so_ + s_ * 1024 + 256);                                      \
      fx1[b] = ld128(xb + so_ + 4096 + (((2 * s_ + hh) ^ sw) << 4));                                           \
      fv[b] = ldtr2((lds_cptr)(uintptr_t)vb0, (lds_cptr)(uintptr_t)vb1);                                       \
      { const unsigned d_ = s_ < 3 ? vstep : ((((t) >> 2) % 3) < 2 ? vfwd : vback); vb0 += d_; vb1 += d_; PIN(vb0); PIN(vb1); } }
      BAR();
      LOAD_A(0, 0)
#pragma unroll
      for (int t = 0; t < 32; ++t) {
        if (t + 1 < 32) {
          if (((t + 1) & 3) == 0) BAR();
          LOAD_A(t + 1, (t + 1) & 1)
        }
        SBAR();
        aqk0 = MFMA(fqk[t & 1], fx0[t & 1], aqk0);
        aqk1 = MFMA(fqk[t & 1], fx1[t & 1], aqk1);
        av0 = MFMA(fx0[t & 1], fv[t & 1], av0);
        av1 = MFMA(fx1[t & 1], fv[t & 1], av1);
        SBAR();
      }
      lds_arrive(lds0 + L_CNT + 32, 1, lane);
      STAMP(1);
      const float qs = 0.25f * 1.4426950408889634f;
      tile = qb * 8 + 2 * wid;
#pragma unroll
      for (int mt = 0; mt < 2; ++mt) {
        const f32x16& aq = mt ? aqk1 : aqk0;
        const f32x16& av = mt ? av1 : av0;
        u32x4 kv, va, vc, qv;
#pragma unroll
        for (int i = 0; i < 4; ++i) {
          kv[i] = cvtpk(aq[8 + 2 * i], aq[8 + 2 * i + 1]);
          va[i] = cvtpk(av[2 * i], av[2 * i + 1]);
          vc[i] = cvtpk(av[8 + 2 * i], av[8 + 2 * i + 1]);
          qv[i] = cvtpk(aq[2 * i] * qs, aq[2 * i + 1] * qs);
        }
        const unsigned kvo = WS_KV + (h * 64 + tile + mt) * 2048;
        const lds_ptr tb = L + (2 * wid + mt) * TILE_B;
        st_sc1(kv, rs, kvo + lane * 16);
        *(LAS u32x4*)(tb + lane * 16) = kv;
        if (!g1) {
          st_sc1(va, rs, kvo + 1024 + hh * 256 + (lane & 15) * 16);
          st_sc1(vc, rs, kvo + 1536 + hh * 256 + (lane & 15) * 16);
          *(LAS u32x4*)(tb + 1024 + hh * 272 + (lane & 15) * 16) = va;
          *(LAS u32x4*)(tb + 1568 + hh * 272 + (lane & 15) * 16) = vc;
        } else if (r32 == 16) {
#pragma unroll
          for (int rq = 0; rq < 4; ++rq)
            *(LAS f32x4*)(L + L_G + (wid * 64 + mt * 32 + 8 * rq + 4 * hh) * 4) = f32x4{av[4 * rq], av[4 * rq + 1], av[4 * rq + 2], av[4 * rq + 3]};
        } else if (r32 < 19) {
          *(LAS u32x4*)(tb + 1024 + (2 * (r32 - 17) + hh) * 272 + 256) = u32x4{0x3f803f80u, 0x3f803f80u, 0x3f803f80u, 0x3f803f80u};
        }
        if (mt == 0) qf = __builtin_bit_cast(bf16x8, qv);
        else *(LAS u32x4*)(L + L_QX + wid * 1024 + lane * 16) = qv;
      }
      lds_arrive(lds0 + L_CNT, 2, lane);
      const u64 tg = make_tag(*(const LAS u64*)(L + L_E));
      asm volatile("s_waitcnt vmcnt(0)" ::: "memory");
      if (lane < 2) __hip_atomic_store((gu64*)(ws + WS_FKV + (h * 64 + tile + lane) * 64), tg, RLX_AGENT);
      lds_wait(cntp, 8);
    }
    const u64 tag = make_tag(*(const LAS u64*)(L + L_E));
    const float g = gate_from_logit(*(const LAS float*)(L + L_G + ((tile & 7) * 32 + r32) * 4));
    const int vrow = r32 < 16 ? r32 : ((r32 >= 20 && r32 < 28) ? 0 : 16);
    const lds_cptr kp = (lds_cptr)L + lane * 16;
    const lds_cptr vp = (lds_cptr)L + 1024 + hh * 272 + vrow * 16;
    f32x16 negm, o = f32x16{}, sa;
    {
      const bf16x8 k0 = ld128(kp);
      const f32x16 s0 = MFMA(k0, qf, f32x16{});
      const float m = swap_max(max16(s0));
#pragma unroll
      for (int r = 0; r < 16; ++r) negm[r] = -m;
      sa = MFMA(k0, qf, negm);
    }
    bf16x8 kf = ld128(kp + TILE_B), v0 = ld128(vp), v1 = bf16x8{}, p1 = bf16x8{};
    unsigned ka = (unsigned)(uintptr_t)kp, va_ = (unsigned)(uintptr_t)vp, ca = lds0 + L_CNT + 4;
    STAMP(2);
#define RUN_CHUNKS(N) { unsigned n_ = (N), t_, sp_;                                                            \
      asm volatile(ATT_ASM                                                                                     \
        : "+{v[64:79]}"(sa), "+{v[112:127]}"(o), "+{v[132:135]}"(kf), "+{v[136:139]}"(v0), "+{v[140:143]}"(v1), \
          "+{v[148:151]}"(p1), "+{v152}"(ka), "+{v153}"(va_), "+{v154}"(ca), [n] "+s"(n_), [tmp] "=&s"(t_), [spin] "=&s"(sp_) \
        : "{v[96:111]}"(negm), "{v[128:131]}"(qf)                                                              \
        : "v80", "v81", "v82", "v83", "v84", "v85", "v86", "v87", "v88", "v89", "v90", "v91", "v92", "v93", "v94", "v95", \
          "v144", "v145", "v146", "v147", "v155", "scc", "memory"); }
    RUN_CHUNKS(4)
    STAMP(9);
    u32x4 wor[8];
    {
      const char* wsrc = wo + (size_t)(wid * 64 + (lane >> 3)) * 1024 + ct * 128 + (lane & 7) * 16;
#pragma unroll
      for (int i = 0; i < 8; ++i) wor[i] = *(const u32x4*)(wsrc + i * 8192);
    }
    SBAR();
    RUN_CHUNKS(4)
    o = MFMA(v1, p1, o);
    float l = hh ? o[12] : o[8];
    bool ok = l < 1.2676506e30f;
#pragma unroll
    for (int r = 0; r < 8; ++r) ok = ok && (fabsf(o[r]) < 3.0e38f);
    if (__builtin_expect(!__all(ok), 0)) {
      float m = -INFINITY;
      o = f32x16{};
#pragma unroll 1
      for (int t = 0; t < 64; ++t) {
        f32x16 z = MFMA(ld128(kp + t * TILE_B), qf, f32x16{});
        const float mn = fmaxf(m, swap_max(max16(z)));
        const float al = __builtin_amdgcn_exp2f(m - mn);
        m = mn;
#pragma unroll
        for (int r = 0; r < 16; ++r) { z[r] = __builtin_amdgcn_exp2f(z[r] - m); o[r] *= al; }
        bf16x8 pa, pb; pack_p(z, pa, pb);
        o = MFMA(ld128(vp + t * TILE_B), pa, o);
        o = MFMA(ld128(vp + t * TILE_B + 544), pb, o);
      }
      l = hh ? o[12] : o[8];
    }
    const float sc = g / l;
    u32x4 ov;
#pragma unroll
    for (int i = 0; i < 4; ++i) ov[i] = cvtpk(o[2 * i] * sc, o[2 * i + 1] * sc);
    st_sc1(ov, rs, WS_A + (tile * KSTEPS + h) * 1024 + lane * 16);
    STAMP(3);
    flag_set(ws, WS_FO + (tile * 32 + h) * 64, tag, lane);
    STAMP(4);
    BAR();
    {
      const int wrow = lane >> 3;
      lds_ptr wd = L + F_WO + (wid * 64 + wrow) * 128 + (((lane & 7) ^ (((wrow >> 1) & 1) << 2)) << 4);
#pragma unroll
      for (int i = 0; i < 8; ++i) *(LAS u32x4*)(wd + i * 1024) = wor[i];
    }
    BAR();
    const int mt = wid & 1, kq = wid >> 1;
    f32x16 fin[2];
#pragma unroll
    for (int c2 = 0; c2 < 2; ++c2)
#pragma unroll
      for (int rq = 0; rq < 4; ++rq) {
        const f32x4 v = *(const LAS f32x4*)(L + F_AUXP + kq * 16384 + ((c2 * 2 + mt) * 4 + rq) * 1024 + lane * 16);
        fin[c2][4 * rq] = v[0]; fin[c2][4 * rq + 1] = v[1]; fin[c2][4 * rq + 2] = v[2]; fin[c2][4 * rq + 3] = v[3];
      }
    bf16x8 wa[8], wb[8];
    {
      const lds_cptr rsrc = (lds_cptr)L + F_WO + (kq * 128 + 4 * hh + q4) * 128 + g1 * 32 + p4 * 8;
      const int wx = ((q4 >> 1) & 1) * 64;
#pragma unroll
      for (int i = 0; i < 8; ++i) {
        wa[i] = ldtr2(rsrc + i * 2048 + wx, rsrc + i * 2048 + 1024 + wx);
        wb[i] = ldtr2(rsrc + i * 2048 + (wx ^ 64), rsrc + i * 2048 + 1024 + (wx ^ 64));
      }
    }
    BAR();
    const int tt = qb * 8 + u * 2 + mt;
    STAMP(5);
    flags_wait(ws, WS_FO + (tt * 32 + kq * 8 + (lane & 7)) * 64, tag);
    STAMP(6);
    u32x4 of[8];
#pragma unroll
    for (int i = 0; i < 8; ++i) of[i] = __builtin_amdgcn_raw_buffer_load_b128(rs, lane * 16 + i * 1024, WS_A + (tt * KSTEPS + kq * 8) * 1024, SC1);
#pragma unroll
    for (int i = 0; i < 8; ++i) {
      fin[0] = MFMA(wa[i], __builtin_bit_cast(bf16x8, of[i]), fin[0]);
      fin[1] = MFMA(wb[i], __builtin_bit_cast(bf16x8, of[i]), fin[1]);
    }
    STAMP(7);
#pragma unroll
    for (int c2 = 0; c2 < 2; ++c2)
#pragma unroll
      for (int rq = 0; rq < 4; ++rq) {
        const f32x4 v = {fin[c2][4 * rq], fin[c2][4 * rq + 1], fin[c2][4 * rq + 2], fin[c2][4 * rq + 3]};
        *(LAS f32x4*)(L + wid * P_WAVE + r32 * P_ROW + (c2 * 32 + 8 * rq + 4 * hh) * 4) = v;
      }
    const int tok = tid >> 3, cg = tid & 7;
    const u32x4 bw = *(const u32x4*)(b2 + (ct * 64 + cg * 8) * 2);
    const float og = bfround(1.0f - gate_from_logit(*(const LAS float*)(L + L_G + (u * 64 + tok) * 4)));
    BAR();
    f32x4 s0 = {0, 0, 0, 0}, s1 = {0, 0, 0, 0};
#pragma unroll
    for (int k = 0; k < 4; ++k) {
      const lds_cptr p = (lds_cptr)L + (k * 2 + (tok >> 5)) * P_WAVE + (tok & 31) * P_ROW + cg * 32;
      s0 += *(const LAS f32x4*)p; s1 += *(const LAS f32x4*)(p + 16);
    }
    u32x4 r;
    r[0] = cvtpk(s0[0] + og * __uint_as_float(bw[0] << 16), s0[1] + og * __uint_as_float(bw[0] & 0xffff0000u));
    r[1] = cvtpk(s0[2] + og * __uint_as_float(bw[1] << 16), s0[3] + og * __uint_as_float(bw[1] & 0xffff0000u));
    r[2] = cvtpk(s1[0] + og * __uint_as_float(bw[2] << 16), s1[1] + og * __uint_as_float(bw[2] & 0xffff0000u));
    r[3] = cvtpk(s1[2] + og * __uint_as_float(bw[3] << 16), s1[3] + og * __uint_as_float(bw[3] & 0xffff0000u));
    *(u32x4*)(out + (size_t)(qb * 256 + u * 64 + tok) * 1024 + (ct * 64 + cg * 8) * 2) = r;
    if (blockIdx.x == 0 && tid == 0) __hip_atomic_store((gu64*)(ws + WS_E), tag, RLX_AGENT);
    STAMP(8);
    STAMP_OUT;
  } else {
    const int v = wid - 8;
    f32x16 acc[4];
#pragma unroll
    for (int ft = 0; ft < 2; ++ft)
#pragma unroll
      for (int rq = 0; rq < 4; ++rq) {
        const u32x2 bw = *(const u32x2*)(b1 + (hf * 64 + ft * 32 + 8 * rq + 4 * hh) * 2);
        acc[2 * ft][4 * rq] = __uint_as_float(bw[0] << 16); acc[2 * ft][4 * rq + 1] = __uint_as_float(bw[0] & 0xffff0000u);
        acc[2 * ft][4 * rq + 2] = __uint_as_float(bw[1] << 16); acc[2 * ft][4 * rq + 3] = __uint_as_float(bw[1] & 0xffff0000u);
      }
    acc[1] = acc[0]; acc[3] = acc[2];
    const int row0 = v * 64 + r32, sw = (row0 >> 1) & 7;
    const lds_cptr xb = (lds_cptr)L + row0 * 128;
    const int w1x = ((q4 >> 1) & 1) * 64;
    const lds_cptr wb0 = (lds_cptr)L + ST_W1 + (8 * hh + q4) * 128 + g1 * 32 + p4 * 8;
    const lds_cptr wba = wb0 + w1x, wbb = wb0 + (w1x ^ 64);
    bf16x8 fx0[2], fx1[2], fa[2], fb[2];
#define LOAD_F(t, b) { const int so_ = (((t) >> 2) % 3) * ST_BYTES, s_ = (t) & 3;                              \
      fx0[b] = ld128(xb + so_ + (((2 * s_ + hh) ^ sw) << 4));                                                  \
      fa[b] = ldtr2(wba + so_ + s_ * 2048, wba + so_ + s_ * 2048 + 512);                                       \
      fx1[b] = ld128(xb + so_ + 4096 + (((2 * s_ + hh) ^ sw) << 4));                                           \
      fb[b] = ldtr2(wbb + so_ + s_ * 2048, wbb + so_ + s_ * 2048 + 512); }
    BAR();
    LOAD_F(0, 0)
#pragma unroll
    for (int t = 0; t < 32; ++t) {
      if (t + 1 < 32) {
        if (((t + 1) & 3) == 0) BAR();
        LOAD_F(t + 1, (t + 1) & 1)
      }
      SBAR();
      acc[0] = MFMA(fa[t & 1], fx0[t & 1], acc[0]);
      acc[1] = MFMA(fa[t & 1], fx1[t & 1], acc[1]);
      acc[2] = MFMA(fb[t & 1], fx0[t & 1], acc[2]);
      acc[3] = MFMA(fb[t & 1], fx1[t & 1], acc[3]);
      SBAR();
    }
    lds_arrive(lds0 + L_CNT + 32, 1, lane);
    STAMP(1);
    __builtin_amdgcn_s_setprio(1);
    const u64 tag = make_tag(*(const LAS u64*)(L + L_E));
    lds_wait(cntp, 8);
    if (h < 28) {
#pragma unroll
      for (int mt = 0; mt < 2; ++mt) {
        const float og = bfround(1.0f - gate_from_logit(*(const LAS float*)(L + L_G + (row0 + mt * 32) * 4)));
        const int tt = qb * 8 + v * 2 + mt;
#pragma unroll
        for (int ft = 0; ft < 2; ++ft)
#pragma unroll
          for (int s = 0; s < 2; ++s) {
            u32x4 hv;
#pragma unroll
            for (int i = 0; i < 4; ++i) {
              const float a = fmaxf(acc[ft * 2 + mt][8 * s + 2 * i], 0.f), b = fmaxf(acc[ft * 2 + mt][8 * s + 2 * i + 1], 0.f);
              hv[i] = cvtpk(a * og * a, b * og * b);
            }
            st_sc1(hv, rs, WS_A + (tt * KSTEPS + 32 + h * 4 + ft * 2 + s) * 1024 + lane * 16);
          }
      }
      flag_set(ws, WS_FH + ((qb * 4 + v) * 32 + h) * 64, tag, lane);
    }
    STAMP(2);
    lds_wait(cntp + 32, 8);
    {
      const lds_ptr kw = L + 2 * v * TILE_B + lane * 16;
      const lds_ptr vw = L + 2 * v * TILE_B + 1024 + (lane >> 4) * 272 + (lane & 15) * 16;
      const lds_ptr ow = L + (2 * v + ((lane >> 2) & 1)) * TILE_B + 1024 + (lane & 3) * 272 + 256;
#define GATHER(C0, NC) {                                                                                        \
      const int fi = lane < 2 * (NC) ? lane : 0;                                                                \
      flags_wait(ws, WS_FKV + (h * 64 + ((qb + (C0) + (fi >> 1)) & 7) * 8 + 2 * v + (fi & 1)) * 64, tag);       \
      u32x4 kr[NC][4];                                                                                          \
      _Pragma("unroll") for (int c = 0; c < (NC); ++c) _Pragma("unroll") for (int i = 0; i < 4; ++i)            \
        kr[c][i] = __builtin_amdgcn_raw_buffer_load_b128(rs, lane * 16 + i * 1024,                              \
                       WS_KV + (h * 64 + ((qb + (C0) + c) & 7) * 8 + 2 * v) * 2048, SC1);                       \
      _Pragma("unroll") for (int c = 0; c < (NC); ++c) {                                                        \
        const int o_ = ((C0) + c) * 8 * TILE_B;                                                                 \
        if (lane < 8) *(LAS u32x4*)(ow + o_) = u32x4{0x3f803f80u, 0x3f803f80u, 0x3f803f80u, 0x3f803f80u};       \
        *(LAS u32x4*)(kw + o_) = kr[c][0]; *(LAS u32x4*)(vw + o_) = kr[c][1];                                   \
        *(LAS u32x4*)(kw + o_ + TILE_B) = kr[c][2]; *(LAS u32x4*)(vw + o_ + TILE_B) = kr[c][3];                 \
        lds_arrive(lds0 + L_CNT + ((C0) + c) * 4, 2, lane);                                                     \
      } }
      GATHER(1, 3)
      STAMP(3);
      GATHER(4, 4)
    }
    STAMP(4);
    flags_wait(ws, WS_FH + ((qb * 4 + u) * 32 + 7 * v + (lane < 7 ? lane : 0)) * 64, tag);
    STAMP(5);
    {
      constexpr int NS = 28, PD = 4;
      const char* wsrc = w2 + (size_t)(v * 448 + (lane >> 3)) * 1024 + ct * 128 + (lane & 7) * 16;
      const unsigned ao = WS_A + ((qb * 8 + u * 2) * KSTEPS + 32 + v * 28) * 1024;
      u32x4 wr[PD][2], ar[PD][2];
#define FETCH2(i, slot) { wr[slot][0] = *(const u32x4*)(wsrc + (i) * 16384); wr[slot][1] = *(const u32x4*)(wsrc + (i) * 16384 + 8192); \
        ar[slot][0] = __builtin_amdgcn_raw_buffer_load_b128(rs, lane * 16, ao + (i) * 1024, SC1);                                        \
        ar[slot][1] = __builtin_amdgcn_raw_buffer_load_b128(rs, lane * 16, ao + (KSTEPS + (i)) * 1024, SC1); }
#pragma unroll
      for (int i = 0; i < PD; ++i) FETCH2(i, i)
#pragma unroll
      for (int i = 0; i < 4; ++i) acc[i] = f32x16{};
      const int wrow = lane >> 3;
      const lds_ptr wd = L + L_SCR + v * 4096 + wrow * 128 + (((lane & 7) ^ (((wrow >> 1) & 1) << 2)) << 4);
      const lds_cptr rsrc = (lds_cptr)L + L_SCR + v * 4096 + (4 * hh + q4) * 128 + g1 * 32 + p4 * 8;
#define PACE() { SBAR(); __builtin_amdgcn_s_sleep(1); SBAR(); }
#pragma unroll 1
      for (int i0 = 0; i0 < NS; i0 += PD) {
#pragma unroll
        for (int j = 0; j < PD; ++j) {
          const int buf = (j & 1) * 2048;
          *(LAS u32x4*)(wd + buf) = wr[j][0];
          *(LAS u32x4*)(wd + buf + 1024) = wr[j][1];
          const bf16x8 wa = ldtr2(rsrc + buf + w1x, rsrc + buf + 1024 + w1x);
          const bf16x8 wb = ldtr2(rsrc + buf + (w1x ^ 64), rsrc + buf + 1024 + (w1x ^ 64));
          const bf16x8 a0 = __builtin_bit_cast(bf16x8, ar[j][0]), a1 = __builtin_bit_cast(bf16x8, ar[j][1]);
          acc[0] = MFMA(wa, a0, acc[0]); PACE()
          acc[1] = MFMA(wa, a1, acc[1]); PACE()
          acc[2] = MFMA(wb, a0, acc[2]); PACE()
          acc[3] = MFMA(wb, a1, acc[3]);
          SBAR();
          const int in = i0 + PD + j < NS ? i0 + PD + j : NS - 1;
          FETCH2(in, j)
          SBAR();
        }
      }
    }
    STAMP(6);
    BAR();
#pragma unroll
    for (int a = 0; a < 4; ++a)
#pragma unroll
      for (int rq = 0; rq < 4; ++rq) {
        const f32x4 t = {acc[a][4 * rq], acc[a][4 * rq + 1], acc[a][4 * rq + 2], acc[a][4 * rq + 3]};
        *(LAS f32x4*)(L + F_AUXP + v * 16384 + (a * 4 + rq) * 1024 + lane * 16) = t;
      }
    BAR();
    STAMP(7);
    STAMP_OUT;
  }
}

extern "C" void kernel_launch(void* const* d_in, const int* in_sizes, int n_in, void* d_out, int out_size,
                              void* d_ws, size_t ws_size, hipStream_t stream) {
  (void)hipFuncSetAttribute((const void*)fused_k, hipFuncAttributeMaxDynamicSharedMemorySize, LDS_BYTES);
  fused_k<<<256, 768, LDS_BYTES, stream>>>((const char*)d_in[0], (const char*)d_in[1], (const char*)d_in[2],
                                           (const char*)d_in[4], (const char*)d_in[5], (const char*)d_in[6],
                                           (const char*)d_in[7], (const char*)d_in[8], (char*)d_ws, (char*)d_out);
}
```

```cpp
#include <hip/hip_runtime.h>
#include <hip/hip_bf16.h>
#include <stdint.h>

typedef __attribute__((ext_vector_type(8))) __bf16 bf16x8;
typedef __attribute__((ext_vector_type(2))) __bf16 bf16x2;
typedef __attribute__((ext_vector_type(4))) short s16x4;
typedef __attribute__((ext_vector_type(8))) short s16x8;
typedef __attribute__((ext_vector_type(16))) float f32x16;
typedef __attribute__((ext_vector_type(4))) float f32x4;
typedef __attribute__((ext_vector_type(4))) unsigned u32x4;
typedef __attribute__((ext_vector_type(2))) unsigned u32x2;
typedef unsigned long long u64;
#define LAS __attribute__((address_space(3)))
#define GAS __attribute__((address_space(1)))
typedef LAS const char* lds_cptr;
typedef LAS char* lds_ptr;
typedef GAS u64 gu64;

#define TOUCH 0
#define AHEAD 2
#define DELAY 0
#define CODEPF 1
#define PAGEPF 0
#define CODE_BACK 0
constexpr int FF = 1792;
constexpr int KSTEPS = 32 + FF / 16;

constexpr unsigned WS_E = 0;
constexpr unsigned WS_FKV = 4096;
constexpr unsigned WS_FO = WS_FKV + 2048 * 64;
constexpr unsigned WS_FH = WS_FO + 2048 * 64;
constexpr unsigned WS_KV = 1u << 20;
constexpr unsigned WS_A = WS_KV + 32u * 64 * 2048;
constexpr unsigned WS_END = WS_A + 64u * KSTEPS * 1024;

constexpr int ST_X = 0, ST_QK = 32768, ST_V = ST_QK + 4096, ST_W1 = ST_V + 2048, ST_BYTES = ST_W1 + 8192;
constexpr int TILE_B = 2112;
constexpr int L_SCR = 64 * TILE_B;
constexpr int L_G4 = L_SCR + 16384;
constexpr int L_QX = L_G4 + 4096;
constexpr int L_G = L_QX + 4096, L_E = L_G + 1024, L_CNT = L_E + 16;
constexpr int L_JUNK = L_CNT + 48;
constexpr int LDS_BYTES = L_JUNK + 256;
constexpr int F_AUXP = 0;
constexpr int F_WO = 65536;
constexpr int P_ROW = 272, P_WAVE = 32 * P_ROW;

#define STAMP(i)
#define STAMP_DECL
#define STAMP_OUT
#define MFMA(a, b, c) __builtin_amdgcn_mfma_f32_32x32x16_bf16(a, b, c, 0, 0, 0)
#define SBAR() __builtin_amdgcn_sched_barrier(0)
#define PIN(x) asm volatile("" : "+v"(x))
#define RLX_AGENT __ATOMIC_RELAXED, __HIP_MEMORY_SCOPE_AGENT
#define SC1 16

__device__ __forceinline__ unsigned cvtpk(float lo, float hi) {
  bf16x2 v; v[0] = (__bf16)lo; v[1] = (__bf16)hi; return __builtin_bit_cast(unsigned, v);
}
__device__ __forceinline__ float bfround(float x) { return (float)(__bf16)x; }
__device__ __forceinline__ bf16x8 ldtr2(lds_cptr p0, lds_cptr p1) {
  s16x4 a = __builtin_amdgcn_ds_read_tr16_b64_v4i16((LAS s16x4*)p0), b = __builtin_amdgcn_ds_read_tr16_b64_v4i16((LAS s16x4*)p1);
  s16x8 r = {a[0], a[1], a[2], a[3], b[0], b[1], b[2], b[3]};
  return __builtin_bit_cast(bf16x8, r);
}
__device__ __forceinline__ bf16x8 ld128(lds_cptr p) { return *(const LAS bf16x8*)p; }
__device__ __forceinline__ void glds16(const char* sbase, unsigned voff, unsigned lds_base) {
  unsigned sv;
  asm volatile("s_mov_b32 %0, m0\n\ts_mov_b32 m0, %3\n\ts_nop 0\n\tglobal_load_lds_dwordx4 %1, %2\n\ts_mov_b32 m0, %0"
               : "=&s"(sv) : "v"(voff), "s"(sbase), "s"(__builtin_amdgcn_readfirstlane(lds_base)) : "memory");
}
__device__ __forceinline__ void touch_lines(const char* sbase, unsigned voff, unsigned lds_base) {
  unsigned sv;
  asm volatile("s_mov_b32 %0, m0\n\ts_mov_b32 m0, %3\n\ts_nop 0\n\tglobal_load_lds_dword %1, %2\n\ts_mov_b32 m0, %0"
               : "=&s"(sv) : "v"(voff), "s"(sbase), "s"(__builtin_amdgcn_readfirstlane(lds_base)) : "memory");
}
__device__ __forceinline__ void st_sc1(u32x4 v, __amdgpu_buffer_rsrc_t rs, unsigned off) { __builtin_amdgcn_raw_buffer_store_b128(v, rs, off, 0, SC1); }
__device__ __forceinline__ float swap_max(float v) {
  auto rr = __builtin_amdgcn_permlane32_swap(__float_as_uint(v), __float_as_uint(v), false, false);
  return fmaxf(__uint_as_float(rr[0]), __uint_as_float(rr[1]));
}
__device__ __forceinline__ float gate_from_logit(float z) {
  z = bfround(z);
  return bfround(1.0f / (1.0f + __expf(-z)));
}
__device__ __forceinline__ float max16(const f32x16& s) {
  float a = fmaxf(fmaxf(s[0], s[1]), s[2]);
#pragma unroll
  for (int r = 3; r < 15; r += 2) a = fmaxf(fmaxf(a, s[r]), s[r + 1]);
  return fmaxf(a, s[15]);
}
__device__ __forceinline__ void pack_p(const f32x16& s, bf16x8& p0, bf16x8& p1) {
  u32x4 a, b;
#pragma unroll
  for (int i = 0; i < 4; ++i) { a[i] = cvtpk(s[2 * i], s[2 * i + 1]); b[i] = cvtpk(s[8 + 2 * i], s[8 + 2 * i + 1]); }
  p0 = __builtin_bit_cast(bf16x8, a); p1 = __builtin_bit_cast(bf16x8, b);
}
__device__ __forceinline__ u64 make_tag(u64 e) {
  const unsigned lo = __builtin_amdgcn_readfirstlane((unsigned)e), hi = __builtin_amdgcn_readfirstlane((unsigned)(e >> 32));
  return (((u64)hi << 32) | lo) * 0x9E3779B97F4A7C15ull + 0xD1B54A32D192ED03ull;
}
__device__ __forceinline__ void lds_arrive(unsigned addr, unsigned n, int lane) {
  asm volatile("s_waitcnt lgkmcnt(0)" ::: "memory");
  if (lane == 0) asm volatile("ds_add_u32 %0, %1" :: "v"(addr), "v"(n) : "memory");
}
constexpr unsigned SPIN_CAP = 1u << 20;
__device__ __forceinline__ void lds_wait(lds_cptr p, unsigned want) {
#pragma unroll 1
  for (unsigned spins = 0; spins < SPIN_CAP; ++spins) {
    const unsigned v = __builtin_amdgcn_readfirstlane(*(const volatile LAS unsigned*)p);
    if (v >= want) break;
    __builtin_amdgcn_s_sleep(1);
  }
  asm volatile("" ::: "memory");
}
__device__ __forceinline__ void flags_wait(const char* ws, unsigned off, u64 tag) {
  gu64* f = (gu64*)(ws + off);
#pragma unroll 1
  for (unsigned spins = 0; spins < (SPIN_CAP >> 4); ++spins) {
    const bool ok = __hip_atomic_load(f, RLX_AGENT) == tag;
    if (__all(ok)) break;
    __builtin_amdgcn_s_sleep(2);
  }
  asm volatile("" ::: "memory");
}
__device__ __forceinline__ void flag_set(char* ws, unsigned off, u64 tag, int lane) {
  asm volatile("s_waitcnt vmcnt(0)" ::: "memory");
  if (lane == 0) __hip_atomic_store((gu64*)(ws + off), tag, RLX_AGENT);
}

#define ATT_ASM \
  "s_nop 15\n" \
  "Latt_loop%=:\n" \
  "s_waitcnt lgkmcnt(2)\n" \
  "v_mfma_f32_32x32x16_bf16 v[80:95], v[132:135], v[128:131], v[96:111]\n" \
  "ds_read_b128 v[132:135], v152 offset:4224\n" \
  "v_exp_f32 v64, v64\n" \
  "v_exp_f32 v65, v65\n" \
  "v_exp_f32 v66, v66\n" \
  "v_exp_f32 v67, v67\n" \
  "s_waitcnt lgkmcnt(2)\n" \
  "v_mfma_f32_32x32x16_bf16 v[112:127], v[140:143], v[148:151], v[112:127]\n" \
  "ds_read_b128 v[140:143], v153 offset:544\n" \
  "v_exp_f32 v68, v68\n" \
  "v_exp_f32 v69, v69\n" \
  "v_exp_f32 v70, v70\n" \
  "v_exp_f32 v71, v71\n" \
  "v_cvt_pk_bf16_f32 v144, v64, v65\n" \
  "v_cvt_pk_bf16_f32 v145, v66, v67\n" \
  "v_cvt_pk_bf16_f32 v146, v68, v69\n" \
  "v_cvt_pk_bf16_f32 v147, v70, v71\n" \
  "v_exp_f32 v72, v72\n" \
  "v_exp_f32 v73, v73\n" \
  "v_exp_f32 v74, v74\n" \
  "v_exp_f32 v75, v75\n" \
  "s_waitcnt lgkmcnt(2)\n" \
  "v_mfma_f32_32x32x16_bf16 v[112:127], v[136:139], v[144:147], v[112:127]\n" \
  "ds_read_b128 v[136:139], v153 offset:2112\n" \
  "v_exp_f32 v76, v76\n" \
  "v_exp_f32 v77, v77\n" \
  "v_exp_f32 v78, v78\n" \
  "v_exp_f32 v79, v79\n" \
  "v_cvt_pk_bf16_f32 v148, v72, v73\n" \
  "v_cvt_pk_bf16_f32 v149, v74, v75\n" \
  "v_cvt_pk_bf16_f32 v150, v76, v77\n" \
  "v_cvt_pk_bf16_f32 v151, v78, v79\n" \
  "s_waitcnt lgkmcnt(2)\n" \
  "v_mfma_f32_32x32x16_bf16 v[64:79], v[132:135], v[128:131], v[96:111]\n" \
  "ds_read_b128 v[132:135], v152 offset:6336\n" \
  "v_exp_f32 v80, v80\n" \
  "v_exp_f32 v81, v81\n" \
  "v_exp_f32 v82, v82\n" \
  "v_exp_f32 v83, v83\n" \
  "s_waitcnt lgkmcnt(2)\n" \
  "v_mfma_f32_32x32x16_bf16 v[112:127], v[140:143], v[148:151], v[112:127]\n" \
  "ds_read_b128 v[140:143], v153 offset:2656\n" \
  "v_exp_f32 v84, v84\n" \
  "v_exp_f32 v85, v85\n" \
  "v_exp_f32 v86, v86\n" \
  "v_exp_f32 v87, v87\n" \
  "v_cvt_pk_bf16_f32 v144, v80, v81\n" \
  "v_cvt_pk_bf16_f32 v145, v82, v83\n" \
  "v_cvt_pk_bf16_f32 v146, v84, v85\n" \
  "v_cvt_pk_bf16_f32 v147, v86, v87\n" \
  "v_exp_f32 v88, v88\n" \
  "v_exp_f32 v89, v89\n" \
  "v_exp_f32 v90, v90\n" \
  "v_exp_f32 v91, v91\n" \
  "s_waitcnt lgkmcnt(2)\n" \
  "v_mfma_f32_32x32x16_bf16 v[112:127], v[136:139], v[144:147], v[112:127]\n" \
  "ds_read_b128 v[136:139], v153 offset:4224\n" \
  "v_exp_f32 v92, v92\n" \
  "v_exp_f32 v93, v93\n" \
  "v_exp_f32 v94, v94\n" \
  "v_exp_f32 v95, v95\n" \
  "v_cvt_pk_bf16_f32 v148, v88, v89\n" \
  "v_cvt_pk_bf16_f32 v149, v90, v91\n" \
  "v_cvt_pk_bf16_f32 v150, v92, v93\n" \
  "v_cvt_pk_bf16_f32 v151, v94, v95\n" \
  "s_waitcnt lgkmcnt(2)\n" \
  "v_mfma_f32_32x32x16_bf16 v[80:95], v[132:135], v[128:131], v[96:111]\n" \
  "ds_read_b128 v[132:135], v152 offset:8448\n" \
  "v_exp_f32 v64, v64\n" \
  "v_exp_f32 v65, v65\n" \
  "v_exp_f32 v66, v66\n" \
  "v_exp_f32 v67, v67\n" \
  "s_waitcnt lgkmcnt(2)\n" \
  "v_mfma_f32_32x32x16_bf16 v[112:127], v[140:143], v[148:151], v[112:127]\n" \
  "ds_read_b128 v[140:143], v153 offset:4768\n" \
  "v_exp_f32 v68, v68\n" \
  "v_exp_f32 v69, v69\n" \
  "v_exp_f32 v70, v70\n" \
  "v_exp_f32 v71, v71\n" \
  "v_cvt_pk_bf16_f32 v144, v64, v65\n" \
  "v_cvt_pk_bf16_f32 v145, v66, v67\n" \
  "v_cvt_pk_bf16_f32 v146, v68, v69\n" \
  "v_cvt_pk_bf16_f32 v147, v70, v71\n" \
  "v_exp_f32 v72, v72\n" \
  "v_exp_f32 v73, v73\n" \
  "v_exp_f32 v74, v74\n" \
  "v_exp_f32 v75, v75\n" \
  "s_waitcnt lgkmcnt(2)\n" \
  "v_mfma_f32_32x32x16_bf16 v[112:127], v[136:139], v[144:147], v[112:127]\n" \
  "ds_read_b128 v[136:139], v153 offset:6336\n" \
  "v_exp_f32 v76, v76\n" \
  "v_exp_f32 v77, v77\n" \
  "v_exp_f32 v78, v78\n" \
  "v_exp_f32 v79, v79\n" \
  "v_cvt_pk_bf16_f32 v148, v72, v73\n" \
  "v_cvt_pk_bf16_f32 v149, v74, v75\n" \
  "v_cvt_pk_bf16_f32 v150, v76, v77\n" \
  "v_cvt_pk_bf16_f32 v151, v78, v79\n" \
  "s_waitcnt lgkmcnt(2)\n" \
  "v_mfma_f32_32x32x16_bf16 v[64:79], v[132:135], v[128:131], v[96:111]\n" \
  "ds_read_b128 v[132:135], v152 offset:10560\n" \
  "v_exp_f32 v80, v80\n" \
  "v_exp_f32 v81, v81\n" \
  "v_exp_f32 v82, v82\n" \
  "v_exp_f32 v83, v83\n" \
  "s_waitcnt lgkmcnt(2)\n" \
  "v_mfma_f32_32x32x16_bf16 v[112:127], v[140:143], v[148:151], v[112:127]\n" \
  "ds_read_b128 v[140:143], v153 offset:6880\n" \
  "v_exp_f32 v84, v84\n" \
  "v_exp_f32 v85, v85\n" \
  "v_exp_f32 v86, v86\n" \
  "v_exp_f32 v87, v87\n" \
  "v_cvt_pk_bf16_f32 v144, v80, v81\n" \
  "v_cvt_pk_bf16_f32 v145, v82, v83\n" \
  "v_cvt_pk_bf16_f32 v146, v84, v85\n" \
  "v_cvt_pk_bf16_f32 v147, v86, v87\n" \
  "v_exp_f32 v88, v88\n" \
  "v_exp_f32 v89, v89\n" \
  "v_exp_f32 v90, v90\n" \
  "v_exp_f32 v91, v91\n" \
  "s_waitcnt lgkmcnt(2)\n" \
  "v_mfma_f32_32x32x16_bf16 v[112:127], v[136:139], v[144:147], v[112:127]\n" \
  "ds_read_b128 v[136:139], v153 offset:8448\n" \
  "v_exp_f32 v92, v92\n" \
  "v_exp_f32 v93, v93\n" \
  "v_exp_f32 v94, v94\n" \
  "v_exp_f32 v95, v95\n" \
  "v_cvt_pk_bf16_f32 v148, v88, v89\n" \
  "v_cvt_pk_bf16_f32 v149, v90, v91\n" \
  "v_cvt_pk_bf16_f32 v150, v92, v93\n" \
  "v_cvt_pk_bf16_f32 v151, v94, v95\n" \
  "s_waitcnt lgkmcnt(2)\n" \
  "v_mfma_f32_32x32x16_bf16 v[80:95], v[132:135], v[128:131], v[96:111]\n" \
  "ds_read_b128 v[132:135], v152 offset:12672\n" \
  "v_exp_f32 v64, v64\n" \
  "v_exp_f32 v65, v65\n" \
  "v_exp_f32 v66, v66\n" \
  "v_exp_f32 v67, v67\n" \
  "s_waitcnt lgkmcnt(2)\n" \
  "v_mfma_f32_32x32x16_bf16 v[112:127], v[140:143], v[148:151], v[112:127]\n" \
  "ds_read_b128 v[140:143], v153 offset:8992\n" \
  "v_exp_f32 v68, v68\n" \
  "v_exp_f32 v69, v69\n" \
  "v_exp_f32 v70, v70\n" \
  "v_exp_f32 v71, v71\n" \
  "v_cvt_pk_bf16_f32 v144, v64, v65\n" \
  "v_cvt_pk_bf16_f32 v145, v66, v67\n" \
  "v_cvt_pk_bf16_f32 v146, v68, v69\n" \
  "v_cvt_pk_bf16_f32 v147, v70, v71\n" \
  "v_exp_f32 v72, v72\n" \
  "v_exp_f32 v73, v73\n" \
  "v_exp_f32 v74, v74\n" \
  "v_exp_f32 v75, v75\n" \
  "s_waitcnt lgkmcnt(2)\n" \
  "v_mfma_f32_32x32x16_bf16 v[112:127], v[136:139], v[144:147], v[112:127]\n" \
  "ds_read_b128 v[136:139], v153 offset:10560\n" \
  "ds_read_b32 v155, v154\n" \
  "v_exp_f32 v76, v76\n" \
  "v_exp_f32 v77, v77\n" \
  "v_exp_f32 v78, v78\n" \
  "v_exp_f32 v79, v79\n" \
  "v_cvt_pk_bf16_f32 v148, v72, v73\n" \
  "v_cvt_pk_bf16_f32 v149, v74, v75\n" \
  "v_cvt_pk_bf16_f32 v150, v76, v77\n" \
  "v_cvt_pk_bf16_f32 v151, v78, v79\n" \
  "s_waitcnt lgkmcnt(2)\n" \
  "v_mfma_f32_32x32x16_bf16 v[64:79], v[132:135], v[128:131], v[96:111]\n" \
  "ds_read_b128 v[132:135], v152 offset:14784\n" \
  "v_exp_f32 v80, v80\n" \
  "v_exp_f32 v81, v81\n" \
  "v_exp_f32 v82, v82\n" \
  "v_exp_f32 v83, v83\n" \
  "s_waitcnt lgkmcnt(2)\n" \
  "v_mfma_f32_32x32x16_bf16 v[112:127], v[140:143], v[148:151], v[112:127]\n" \
  "ds_read_b128 v[140:143], v153 offset:11104\n" \
  "v_exp_f32 v84, v84\n" \
  "v_exp_f32 v85, v85\n" \
  "v_exp_f32 v86, v86\n" \
  "v_exp_f32 v87, v87\n" \
  "v_cvt_pk_bf16_f32 v144, v80, v81\n" \
  "v_cvt_pk_bf16_f32 v145, v82, v83\n" \
  "v_cvt_pk_bf16_f32 v146, v84, v85\n" \
  "v_cvt_pk_bf16_f32 v147, v86, v87\n" \
  "v_exp_f32 v88, v88\n" \
  "v_exp_f32 v89, v89\n" \
  "v_exp_f32 v90, v90\n" \
  "v_exp_f32 v91, v91\n" \
  "s_waitcnt lgkmcnt(2)\n" \
  "v_mfma_f32_32x32x16_bf16 v[112:127], v[136:139], v[144:147], v[112:127]\n" \
  "ds_read_b128 v[136:139], v153 offset:12672\n" \
  "v_exp_f32 v92, v92\n" \
  "v_exp_f32 v93, v93\n" \
  "v_exp_f32 v94, v94\n" \
  "v_exp_f32 v95, v95\n" \
  "v_cvt_pk_bf16_f32 v148, v88, v89\n" \
  "v_cvt_pk_bf16_f32 v149, v90, v91\n" \
  "v_cvt_pk_bf16_f32 v150, v92, v93\n" \
  "v_cvt_pk_bf16_f32 v151, v94, v95\n" \
  "v_readfirstlane_b32 %[tmp], v155\n" \
  "s_cmp_ge_u32 %[tmp], 8\n" \
  "s_cbranch_scc1 Latt_ok%=\n" \
  "s_mov_b32 %[spin], 0x100000\n" \
  "Latt_spin%=:\n" \
  "s_sleep 1\n" \
  "ds_read_b32 v155, v154\n" \
  "s_waitcnt lgkmcnt(0)\n" \
  "v_readfirstlane_b32 %[tmp], v155\n" \
  "s_sub_u32 %[spin], %[spin], 1\n" \
  "s_cmp_eq_u32 %[spin], 0\n" \
  "s_cbranch_scc1 Latt_ok%=\n" \
  "s_cmp_lt_u32 %[tmp], 8\n" \
  "s_cbranch_scc1 Latt_spin%=\n" \
  "Latt_ok%=:\n" \
  "s_waitcnt lgkmcnt(2)\n" \
  "v_mfma_f32_32x32x16_bf16 v[80:95], v[132:135], v[128:131], v[96:111]\n" \
  "ds_read_b128 v[132:135], v152 offset:16896\n" \
  "v_exp_f32 v64, v64\n" \
  "v_exp_f32 v65, v65\n" \
  "v_exp_f32 v66, v66\n" \
  "v_exp_f32 v67, v67\n" \
  "s_waitcnt lgkmcnt(2)\n" \
  "v_mfma_f32_32x32x16_bf16 v[112:127], v[140:143], v[148:151], v[112:127]\n" \
  "ds_read_b128 v[140:143], v153 offset:13216\n" \
  "v_exp_f32 v68, v68\n" \
  "v_exp_f32 v69, v69\n" \
  "v_exp_f32 v70, v70\n" \
  "v_exp_f32 v71, v71\n" \
  "v_cvt_pk_bf16_f32 v144, v64, v65\n" \
  "v_cvt_pk_bf16_f32 v145, v66, v67\n" \
  "v_cvt_pk_bf16_f32 v146, v68, v69\n" \
  "v_cvt_pk_bf16_f32 v147, v70, v71\n" \
  "v_exp_f32 v72, v72\n" \
  "v_exp_f32 v73, v73\n" \
  "v_exp_f32 v74, v74\n" \
  "v_exp_f32 v75, v75\n" \
  "s_waitcnt lgkmcnt(2)\n" \
  "v_mfma_f32_32x32x16_bf16 v[112:127], v[136:139], v[144:147], v[112:127]\n" \
  "ds_read_b128 v[136:139], v153 offset:14784\n" \
  "v_exp_f32 v76, v76\n" \
  "v_exp_f32 v77, v77\n" \
  "v_exp_f32 v78, v78\n" \
  "v_exp_f32 v79, v79\n" \
  "v_cvt_pk_bf16_f32 v148, v72, v73\n" \
  "v_cvt_pk_bf16_f32 v149, v74, v75\n" \
  "v_cvt_pk_bf16_f32 v150, v76, v77\n" \
  "v_cvt_pk_bf16_f32 v151, v78, v79\n" \
  "s_waitcnt lgkmcnt(2)\n" \
  "v_mfma_f32_32x32x16_bf16 v[64:79], v[132:135], v[128:131], v[96:111]\n" \
  "ds_read_b128 v[132:135], v152 offset:19008\n" \
  "v_exp_f32 v80, v80\n" \
  "v_exp_f32 v81, v81\n" \
  "v_exp_f32 v82, v82\n" \
  "v_exp_f32 v83, v83\n" \
  "s_waitcnt lgkmcnt(2)\n" \
  "v_mfma_f32_32x32x16_bf16 v[112:127], v[140:143], v[148:151], v[112:127]\n" \
  "ds_read_b128 v[140:143], v153 offset:15328\n" \
  "v_exp_f32 v84, v84\n" \
  "v_exp_f32 v85, v85\n" \
  "v_exp_f32 v86, v86\n" \
  "v_exp_f32 v87, v87\n" \
  "v_cvt_pk_bf16_f32 v144, v80, v81\n" \
  "v_cvt_pk_bf16_f32 v145, v82, v83\n" \
  "v_cvt_pk_bf16_f32 v146, v84, v85\n" \
  "v_cvt_pk_bf16_f32 v147, v86, v87\n" \
  "v_exp_f32 v88, v88\n" \
  "v_exp_f32 v89, v89\n" \
  "v_exp_f32 v90, v90\n" \
  "v_exp_f32 v91, v91\n" \
  "s_waitcnt lgkmcnt(2)\n" \
  "v_mfma_f32_32x32x16_bf16 v[112:127], v[136:139], v[144:147], v[112:127]\n" \
  "ds_read_b128 v[136:139], v153 offset:16896\n" \
  "v_exp_f32 v92, v92\n" \
  "v_exp_f32 v93, v93\n" \
  "v_exp_f32 v94, v94\n" \
  "v_exp_f32 v95, v95\n" \
  "v_cvt_pk_bf16_f32 v148, v88, v89\n" \
  "v_cvt_pk_bf16_f32 v149, v90, v91\n" \
  "v_cvt_pk_bf16_f32 v150, v92, v93\n" \
  "v_cvt_pk_bf16_f32 v151, v94, v95\n" \
  "v_add_u32 v152, 16896, v152\n" \
  "v_add_u32 v153, 16896, v153\n" \
  "v_add_u32 v154, 4, v154\n" \
  "s_sub_u32 %[n], %[n], 1\n" \
  "s_cmp_lg_u32 %[n], 0\n" \
  "s_cbranch_scc1 Latt_loop%=\n" \
  "s_waitcnt lgkmcnt(0)\n" \
  "s_nop 7\n"

__global__ __launch_bounds__(768, 3) void fused_k(const char* __restrict__ x, const char* __restrict__ wqkv,
                                                  const char* __restrict__ wo, const char* __restrict__ w1,
                                                  const char* __restrict__ b1, const char* __restrict__ w2,
                                                  const char* __restrict__ b2, const char* __restrict__ gw,
                                                  char* ws, char* __restrict__ out) {
  extern __shared__ __attribute__((aligned(16))) char smem[];
  const int tid = threadIdx.x, lane = tid & 63;
  const int wid = __builtin_amdgcn_readfirstlane(tid >> 6);
  const int xg = blockIdx.x & 7, ig = blockIdx.x >> 3;
  const int qb = (xg & 1) * 4 + (ig & 3), h = (xg >> 1) * 8 + (ig >> 2);
  const int hf = h < 28 ? h : 27;
  const unsigned lds0 = __builtin_amdgcn_readfirstlane((unsigned)(uintptr_t)smem);
  lds_ptr L = (lds_ptr)smem;
  const __amdgpu_buffer_rsrc_t rs = __builtin_amdgcn_make_buffer_rsrc(ws, 0, WS_END, 0x00020000);
  const int r32 = lane & 31, hh = lane >> 5, g1 = (lane >> 4) & 1, q4 = (lane >> 2) & 3, p4 = lane & 3;
  const int u = h >> 3, ct = h & 7;
  const lds_cptr cntp = (lds_cptr)L + L_CNT;
  u64 pc; asm volatile("s_getpc_b64 %0" : "=s"(pc));
  pc &= ~127ull;
  STAMP_DECL
  STAMP(0);
#define BAR() asm volatile("s_waitcnt lgkmcnt(0)\n\ts_barrier" ::: "memory")

  if (wid < 8) {
    bf16x8 qf;
    int tile;
    if (wid >= 4) {
      const int j = wid - 4;
      const char* xs = x + (size_t)(qb * 256 + j * 64) * 1024;
      const unsigned xv0 = (lane >> 3) * 1024 + (((lane & 7) ^ (lane >> 4)) << 4), xv1 = (lane >> 3) * 1024 + (((lane & 7) ^ ((lane >> 4) + 4)) << 4);
      const char* qs_ = wqkv + h * 32 + (size_t)j * 16 * 3072;
      const unsigned qv_ = (lane >> 2) * 3072 + ((lane >> 1) & 1) * 1024 + (lane & 1) * 16;
      const unsigned vv_ = ((lane & 31) >> 1) * 3072 + 2048 + (lane & 1) * 16;
      const char* w1s = w1 + hf * 128 + (size_t)j * 16 * (FF * 2);
      const unsigned w1v = (lane >> 3) * (FF * 2) + (((lane & 7) ^ (((lane >> 4) & 1) << 2)) << 4);
#define ISSUE(c) { const unsigned sb_ = lds0 + ((c) % 3) * ST_BYTES;                                          \
        glds16(qs_ + (size_t)(c) * 64 * 3072, qv_, sb_ + ST_QK + j * 1024);                                   \
        if (lane < 32) glds16(qs_ + (size_t)(c) * 64 * 3072, vv_, sb_ + ST_V + j * 512);                      \
        _Pragma("unroll") for (int i = 0; i < 8; ++i)                                                         \
          glds16(xs + (c) * 128 + i * 8192, (i & 1) ? xv1 : xv0, sb_ + ST_X + (j * 8 + i) * 1024);            \
        glds16(w1s + (size_t)(c) * 64 * (FF * 2), w1v, sb_ + ST_W1 + j * 2048);                               \
        glds16(w1s + (size_t)((c) * 64 + 8) * (FF * 2), w1v, sb_ + ST_W1 + j * 2048 + 1024); }
      ISSUE(0) ISSUE(1)
      STAMP(18);
#pragma unroll
      for (int c = 0; c < 8; ++c) {
        if (c == 0 && AHEAD == 3) asm volatile("s_waitcnt vmcnt(24)\n\ts_barrier" ::: "memory");
        else if (c < 7) asm volatile("s_waitcnt vmcnt(12)\n\ts_barrier" ::: "memory");
        else asm volatile("s_waitcnt vmcnt(0)\n\ts_barrier" ::: "memory");
        STAMP(10 + c);
        if (c >= (AHEAD == 3) && c + 2 < 8) ISSUE(c + 2)
      }
      STAMP(1);
      tile = qb * 8 + 2 * j + 1;
      lds_wait(cntp, 8);
      qf = ld128((lds_cptr)L + L_QX + j * 1024 + lane * 16);
    } else {
      if (wid == 2) {
        if (lane < 6) touch_lines((const char*)pc, (ig * 6 + lane) * 128, lds0 + L_JUNK);
      } else
      if (wid == 0) {
        const u32x4 gv = *(const u32x4*)(gw + lane * 16);
#pragma unroll
        for (int i = 0; i < 4; ++i) {
          *(LAS unsigned short*)(L + L_G4 + lane * 64 + i * 16) = (unsigned short)gv[i];
          *(LAS unsigned short*)(L + L_G4 + lane * 64 + i * 16 + 8) = (unsigned short)(gv[i] >> 16);
        }
      } else if (wid == 1) {
        if (lane == 0) *(LAS u64*)(L + L_E) = __hip_atomic_load((gu64*)(ws + WS_E), RLX_AGENT);
        if (lane < 12) *(LAS unsigned*)(L + L_CNT + lane * 4) = 0u;
      }
      const int row0 = wid * 64 + r32, sw = (row0 >> 1) & 7;
      const lds_cptr xb = (lds_cptr)L + row0 * 128;
      const lds_cptr qkb = (lds_cptr)L + ST_QK + (8 * hh + q4) * 64 + g1 * 32 + p4 * 8;
      unsigned vb0 = lds0 + (g1 ? L_G4 + (8 * hh + q4) * 8 : ST_V + (8 * hh + q4) * 32 + p4 * 8), vb1 = vb0 + (g1 ? 32 : 128);
      unsigned vstep = g1 ? 128 : 512, vfwd = g1 ? 128 : ST_BYTES - 1536, vback = g1 ? 128 : -2 * ST_BYTES - 1536;
      PIN(vstep); PIN(vfwd); PIN(vback);
      f32x16 aqk0 = f32x16{}, aqk1 = f32x16{}, av0 = f32x16{}, av1 = f32x16{};
      bf16x8 fx0[2], fx1[2], fqk[2], fv[2];
#define LOAD_A(t, b) { const int so_ = (((t) >> 2) % 3) * ST_BYTES, s_ = (t) & 3;                              \
      fx0[b] = ld128(xb + so_ + (((2 * s_ + hh) ^ sw) << 4));                                                  \
      fqk[b] = ldtr2(qkb + so_ + s_ * 1024, qkb + so_ + s_ * 1024 + 256);                                      \
      fx1[b] = ld128(xb + so_ + 4096 + (((2 * s_ + hh) ^ sw) << 4));                                           \
      fv[b] = ldtr2((lds_cptr)(uintptr_t)vb0, (lds_cptr)(uintptr_t)vb1);                                       \
      { const unsigned d_ = s_ < 3 ? vstep : ((((t) >> 2) % 3) < 2 ? vfwd : vback); vb0 += d_; vb1 += d_; PIN(vb0); PIN(vb1); } }
      BAR();
      LOAD_A(0, 0)
#pragma unroll
      for (int t = 0; t < 32; ++t) {
        if (t + 1 < 32) {
          if (((t + 1) & 3) == 0) BAR();
          LOAD_A(t + 1, (t + 1) & 1)
        }
        SBAR();
        aqk0 = MFMA(fqk[t & 1], fx0[t & 1], aqk0);
        aqk1 = MFMA(fqk[t & 1], fx1[t & 1], aqk1);
        av0 = MFMA(fx0[t & 1], fv[t & 1], av0);
        av1 = MFMA(fx1[t & 1], fv[t & 1], av1);
        SBAR();
      }
      lds_arrive(lds0 + L_CNT + 32, 1, lane);
      STAMP(1);
      const float qs = 0.25f * 1.4426950408889634f;
      tile = qb * 8 + 2 * wid;
#pragma unroll
      for (int mt = 0; mt < 2; ++mt) {
        const f32x16& aq = mt ? aqk1 : aqk0;
        const f32x16& av = mt ? av1 : av0;
        u32x4 kv, va, vc, qv;
#pragma unroll
        for (int i = 0; i < 4; ++i) {
          kv[i] = cvtpk(aq[8 + 2 * i], aq[8 + 2 * i + 1]);
          va[i] = cvtpk(av[2 * i], av[2 * i + 1]);
          vc[i] = cvtpk(av[8 + 2 * i], av[8 + 2 * i + 1]);
          qv[i] = cvtpk(aq[2 * i] * qs, aq[2 * i + 1] * qs);
        }
        const unsigned kvo = WS_KV + (h * 64 + tile + mt) * 2048;
        const lds_ptr tb = L + (2 * wid + mt) * TILE_B;
        st_sc1(kv, rs, kvo + lane * 16);
        *(LAS u32x4*)(tb + lane * 16) = kv;
        if (!g1) {
          st_sc1(va, rs, kvo + 1024 + hh * 256 + (lane & 15) * 16);
          st_sc1(vc, rs, kvo + 1536 + hh * 256 + (lane & 15) * 16);
          *(LAS u32x4*)(tb + 1024 + hh * 272 + (lane & 15) * 16) = va;
          *(LAS u32x4*)(tb + 1568 + hh * 272 + (lane & 15) * 16) = vc;
        } else if (r32 == 16) {
#pragma unroll
          for (int rq = 0; rq < 4; ++rq)
            *(LAS f32x4*)(L + L_G + (wid * 64 + mt * 32 + 8 * rq + 4 * hh) * 4) = f32x4{av[4 * rq], av[4 * rq + 1], av[4 * rq + 2], av[4 * rq + 3]};
        } else if (r32 < 19) {
          *(LAS u32x4*)(tb + 1024 + (2 * (r32 - 17) + hh) * 272 + 256) = u32x4{0x3f803f80u, 0x3f803f80u, 0x3f803f80u, 0x3f803f80u};
        }
        if (mt == 0) qf = __builtin_bit_cast(bf16x8, qv);
        else *(LAS u32x4*)(L + L_QX + wid * 1024 + lane * 16) = qv;
      }
      lds_arrive(lds0 + L_CNT, 2, lane);
      const u64 tg = make_tag(*(const LAS u64*)(L + L_E));
      asm volatile("s_waitcnt vmcnt(0)" ::: "memory");
      if (lane < 2) __hip_atomic_store((gu64*)(ws + WS_FKV + (h * 64 + tile + lane) * 64), tg, RLX_AGENT);
      lds_wait(cntp, 8);
    }
    const u64 tag = make_tag(*(const LAS u64*)(L + L_E));
    const float g = gate_from_logit(*(const LAS float*)(L + L_G + ((tile & 7) * 32 + r32) * 4));
    const int vrow = r32 < 16 ? r32 : ((r32 >= 20 && r32 < 28) ? 0 : 16);
    const lds_cptr kp = (lds_cptr)L + lane * 16;
    const lds_cptr vp = (lds_cptr)L + 1024 + hh * 272 + vrow * 16;
    f32x16 negm, o = f32x16{}, sa;
    {
      const bf16x8 k0 = ld128(kp);
      const f32x16 s0 = MFMA(k0, qf, f32x16{});
      const float m = swap_max(max16(s0));
#pragma unroll
      for (int r = 0; r < 16; ++r) negm[r] = -m;
      sa = MFMA(k0, qf, negm);
    }
    bf16x8 kf = ld128(kp + TILE_B), v0 = ld128(vp), v1 = bf16x8{}, p1 = bf16x8{};
    unsigned ka = (unsigned)(uintptr_t)kp, va_ = (unsigned)(uintptr_t)vp, ca = lds0 + L_CNT + 4;
    STAMP(2);
#define RUN_CHUNKS(N) { unsigned n_ = (N), t_, sp_;                                                            \
      asm volatile(ATT_ASM                                                                                     \
        : "+{v[64:79]}"(sa), "+{v[112:127]}"(o), "+{v[132:135]}"(kf), "+{v[136:139]}"(v0), "+{v[140:143]}"(v1), \
          "+{v[148:151]}"(p1), "+{v152}"(ka), "+{v153}"(va_), "+{v154}"(ca), [n] "+s"(n_), [tmp] "=&s"(t_), [spin] "=&s"(sp_) \
        : "{v[96:111]}"(negm), "{v[128:131]}"(qf)                                                              \
        : "v80", "v81", "v82", "v83", "v84", "v85", "v86", "v87", "v88", "v89", "v90", "v91", "v92", "v93", "v94", "v95", \
          "v144", "v145", "v146", "v147", "v155", "scc", "memory"); }
    RUN_CHUNKS(4)
    STAMP(9);
    u32x4 wor[8];
    {
      const char* wsrc = wo + (size_t)(wid * 64 + (lane >> 3)) * 1024 + ct * 128 + (lane & 7) * 16;
#pragma unroll
      for (int i = 0; i < 8; ++i) wor[i] = *(const u32x4*)(wsrc + i * 8192);
    }
    SBAR();
    RUN_CHUNKS(4)
    o = MFMA(v1, p1, o);
    float l = hh ? o[12] : o[8];
    bool ok = l < 1.2676506e30f;
#pragma unroll
    for (int r = 0; r < 8; ++r) ok = ok && (fabsf(o[r]) < 3.0e38f);
    if (__builtin_expect(!__all(ok), 0)) {
      float m = -INFINITY;
      o = f32x16{};
#pragma unroll 1
      for (int t = 0; t < 64; ++t) {
        f32x16 z = MFMA(ld128(kp + t * TILE_B), qf, f32x16{});
        const float mn = fmaxf(m, swap_max(max16(z)));
        const float al = __builtin_amdgcn_exp2f(m - mn);
        m = mn;
#pragma unroll
        for (int r = 0; r < 16; ++r) { z[r] = __builtin_amdgcn_exp2f(z[r] - m); o[r] *= al; }
        bf16x8 pa, pb; pack_p(z, pa, pb);
        o = MFMA(ld128(vp + t * TILE_B), pa, o);
        o = MFMA(ld128(vp + t * TILE_B + 544), pb, o);
      }
      l = hh ? o[12] : o[8];
    }
    const float sc = g / l;
    u32x4 ov;
#pragma unroll
    for (int i = 0; i < 4; ++i) ov[i] = cvtpk(o[2 * i] * sc, o[2 * i + 1] * sc);
    st_sc1(ov, rs, WS_A + (tile * KSTEPS + h) * 1024 + lane * 16);
    STAMP(3);
    flag_set(ws, WS_FO + (tile * 32 + h) * 64, tag, lane);
    STAMP(4);
    BAR();
    {
      const int wrow = lane >> 3;
      lds_ptr wd = L + F_WO + (wid * 64 + wrow) * 128 + (((lane & 7) ^ (((wrow >> 1) & 1) << 2)) << 4);
#pragma unroll
      for (int i = 0; i < 8; ++i) *(LAS u32x4*)(wd + i * 1024) = wor[i];
    }
    BAR();
    const int mt = wid & 1, kq = wid >> 1;
    f32x16 fin[2];
#pragma unroll
    for (int c2 = 0; c2 < 2; ++c2)
#pragma unroll
      for (int rq = 0; rq < 4; ++rq) {
        const f32x4 v = *(const LAS f32x4*)(L + F_AUXP + kq * 16384 + ((c2 * 2 + mt) * 4 + rq) * 1024 + lane * 16);
        fin[c2][4 * rq] = v[0]; fin[c2][4 * rq + 1] = v[1]; fin[c2][4 * rq + 2] = v[2]; fin[c2][4 * rq + 3] = v[3];
      }
    bf16x8 wa[8], wb[8];
    {
      const lds_cptr rsrc = (lds_cptr)L + F_WO + (kq * 128 + 4 * hh + q4) * 128 + g1 * 32 + p4 * 8;
      const int wx = ((q4 >> 1) & 1) * 64;
#pragma unroll
      for (int i = 0; i < 8; ++i) {
        wa[i] = ldtr2(rsrc + i * 2048 + wx, rsrc + i * 2048 + 1024 + wx);
        wb[i] = ldtr2(rsrc + i * 2048 + (wx ^ 64), rsrc + i * 2048 + 1024 + (wx ^ 64));
      }
    }
    BAR();
    const int tt = qb * 8 + u * 2 + mt;
    STAMP(5);
    flags_wait(ws, WS_FO + (tt * 32 + kq * 8 + (lane & 7)) * 64, tag);
    STAMP(6);
    u32x4 of[8];
#pragma unroll
    for (int i = 0; i < 8; ++i) of[i] = __builtin_amdgcn_raw_buffer_load_b128(rs, lane * 16 + i * 1024, WS_A + (tt * KSTEPS + kq * 8) * 1024, SC1);
#pragma unroll
    for (int i = 0; i < 8; ++i) {
      fin[0] = MFMA(wa[i], __builtin_bit_cast(bf16x8, of[i]), fin[0]);
      fin[1] = MFMA(wb[i], __builtin_bit_cast(bf16x8, of[i]), fin[1]);
    }
    STAMP(7);
#pragma unroll
    for (int c2 = 0; c2 < 2; ++c2)
#pragma unroll
      for (int rq = 0; rq < 4; ++rq) {
        const f32x4 v = {fin[c2][4 * rq], fin[c2][4 * rq + 1], fin[c2][4 * rq + 2], fin[c2][4 * rq + 3]};
        *(LAS f32x4*)(L + wid * P_WAVE + r32 * P_ROW + (c2 * 32 + 8 * rq + 4 * hh) * 4) = v;
      }
    const int tok = tid >> 3, cg = tid & 7;
    const u32x4 bw = *(const u32x4*)(b2 + (ct * 64 + cg * 8) * 2);
    const float og = bfround(1.0f - gate_from_logit(*(const LAS float*)(L + L_G + (u * 64 + tok) * 4)));
    BAR();
    f32x4 s0 = {0, 0, 0, 0}, s1 = {0, 0, 0, 0};
#pragma unroll
    for (int k = 0; k < 4; ++k) {
      const lds_cptr p = (lds_cptr)L + (k * 2 + (tok >> 5)) * P_WAVE + (tok & 31) * P_ROW + cg * 32;
      s0 += *(const LAS f32x4*)p; s1 += *(const LAS f32x4*)(p + 16);
    }
    u32x4 r;
    r[0] = cvtpk(s0[0] + og * __uint_as_float(bw[0] << 16), s0[1] + og * __uint_as_float(bw[0] & 0xffff0000u));
    r[1] = cvtpk(s0[2] + og * __uint_as_float(bw[1] << 16), s0[3] + og * __uint_as_float(bw[1] & 0xffff0000u));
    r[2] = cvtpk(s1[0] + og * __uint_as_float(bw[2] << 16), s1[1] + og * __uint_as_float(bw[2] & 0xffff0000u));
    r[3] = cvtpk(s1[2] + og * __uint_as_float(bw[3] << 16), s1[3] + og * __uint_as_float(bw[3] & 0xffff0000u));
    *(u32x4*)(out + (size_t)(qb * 256 + u * 64 + tok) * 1024 + (ct * 64 + cg * 8) * 2) = r;
    if (blockIdx.x == 0 && tid == 0) __hip_atomic_store((gu64*)(ws + WS_E), tag, RLX_AGENT);
    STAMP(8);
    STAMP_OUT;
  } else {
    const int v = wid - 8;
    f32x16 acc[4];
#pragma unroll
    for (int ft = 0; ft < 2; ++ft)
#pragma unroll
      for (int rq = 0; rq < 4; ++rq) {
        const u32x2 bw = *(const u32x2*)(b1 + (hf * 64 + ft * 32 + 8 * rq + 4 * hh) * 2);
        acc[2 * ft][4 * rq] = __uint_as_float(bw[0] << 16); acc[2 * ft][4 * rq + 1] = __uint_as_float(bw[0] & 0xffff0000u);
        acc[2 * ft][4 * rq + 2] = __uint_as_float(bw[1] << 16); acc[2 * ft][4 * rq + 3] = __uint_as_float(bw[1] & 0xffff0000u);
      }
    acc[1] = acc[0]; acc[3] = acc[2];
    const int row0 = v * 64 + r32, sw = (row0 >> 1) & 7;
    const lds_cptr xb = (lds_cptr)L + row0 * 128;
    const int w1x = ((q4 >> 1) & 1) * 64;
    const lds_cptr wb0 = (lds_cptr)L + ST_W1 + (8 * hh + q4) * 128 + g1 * 32 + p4 * 8;
    const lds_cptr wba = wb0 + w1x, wbb = wb0 + (w1x ^ 64);
    bf16x8 fx0[2], fx1[2], fa[2], fb[2];
#define LOAD_F(t, b) { const int so_ = (((t) >> 2) % 3) * ST_BYTES, s_ = (t) & 3;                              \
      fx0[b] = ld128(xb + so_ + (((2 * s_ + hh) ^ sw) << 4));                                                  \
      fa[b] = ldtr2(wba + so_ + s_ * 2048, wba + so_ + s_ * 2048 + 512);                                       \
      fx1[b] = ld128(xb + so_ + 4096 + (((2 * s_ + hh) ^ sw) << 4));                                           \
      fb[b] = ldtr2(wbb + so_ + s_ * 2048, wbb + so_ + s_ * 2048 + 512); }
    BAR();
    LOAD_F(0, 0)
#pragma unroll
    for (int t = 0; t < 32; ++t) {
      if (t + 1 < 32) {
        if (((t + 1) & 3) == 0) BAR();
        LOAD_F(t + 1, (t + 1) & 1)
      }
      SBAR();
      acc[0] = MFMA(fa[t & 1], fx0[t & 1], acc[0]);
      acc[1] = MFMA(fa[t & 1], fx1[t & 1], acc[1]);
      acc[2] = MFMA(fb[t & 1], fx0[t & 1], acc[2]);
      acc[3] = MFMA(fb[t & 1], fx1[t & 1], acc[3]);
      SBAR();
    }
    lds_arrive(lds0 + L_CNT + 32, 1, lane);
    STAMP(1);
    __builtin_amdgcn_s_setprio(1);
    const u64 tag = make_tag(*(const LAS u64*)(L + L_E));
    lds_wait(cntp, 8);
    if (h < 28) {
#pragma unroll
      for (int mt = 0; mt < 2; ++mt) {
        const float og = bfround(1.0f - gate_from_logit(*(const LAS float*)(L + L_G + (row0 + mt * 32) * 4)));
        const int tt = qb * 8 + v * 2 + mt;
#pragma unroll
        for (int ft = 0; ft < 2; ++ft)
#pragma unroll
          for (int s = 0; s < 2; ++s) {
            u32x4 hv;
#pragma unroll
            for (int i = 0; i < 4; ++i) {
              const float a = fmaxf(acc[ft * 2 + mt][8 * s + 2 * i], 0.f), b = fmaxf(acc[ft * 2 + mt][8 * s + 2 * i + 1], 0.f);
              hv[i] = cvtpk(a * og * a, b * og * b);
            }
            st_sc1(hv, rs, WS_A + (tt * KSTEPS + 32 + h * 4 + ft * 2 + s) * 1024 + lane * 16);
          }
      }
      flag_set(ws, WS_FH + ((qb * 4 + v) * 32 + h) * 64, tag, lane);
    }
    STAMP(2);
    lds_wait(cntp + 32, 8);
    {
      const lds_ptr kw = L + 2 * v * TILE_B + lane * 16;
      const lds_ptr vw = L + 2 * v * TILE_B + 1024 + (lane >> 4) * 272 + (lane & 15) * 16;
      const lds_ptr ow = L + (2 * v + ((lane >> 2) & 1)) * TILE_B + 1024 + (lane & 3) * 272 + 256;
#define GATHER(C0, NC) {                                                                                        \
      const int fi = lane < 2 * (NC) ? lane : 0;                                                                \
      flags_wait(ws, WS_FKV + (h * 64 + ((qb + (C0) + (fi >> 1)) & 7) * 8 + 2 * v + (fi & 1)) * 64, tag);       \
      u32x4 kr[NC][4];                                                                                          \
      _Pragma("unroll") for (int c = 0; c < (NC); ++c) _Pragma("unroll") for (int i = 0; i < 4; ++i)            \
        kr[c][i] = __builtin_amdgcn_raw_buffer_load_b128(rs, lane * 16 + i * 1024,                              \
                       WS_KV + (h * 64 + ((qb + (C0) + c) & 7) * 8 + 2 * v) * 2048, SC1);                       \
      _Pragma("unroll") for (int c = 0; c < (NC); ++c) {                                                        \
        const int o_ = ((C0) + c) * 8 * TILE_B;                                                                 \
        if (lane < 8) *(LAS u32x4*)(ow + o_) = u32x4{0x3f803f80u, 0x3f803f80u, 0x3f803f80u, 0x3f803f80u};       \
        *(LAS u32x4*)(kw + o_) = kr[c][0]; *(LAS u32x4*)(vw + o_) = kr[c][1];                                   \
        *(LAS u32x4*)(kw + o_ + TILE_B) = kr[c][2]; *(LAS u32x4*)(vw + o_ + TILE_B) = kr[c][3];                 \
        lds_arrive(lds0 + L_CNT + ((C0) + c) * 4, 2, lane);                                                     \
      } }
      GATHER(1, 3)
      STAMP(3);
      GATHER(4, 4)
    }
    STAMP(4);
    flags_wait(ws, WS_FH + ((qb * 4 + u) * 32 + 7 * v + (lane < 7 ? lane : 0)) * 64, tag);
    STAMP(5);
    {
      constexpr int NS = 28, PD = 4;
      const char* wsrc = w2 + (size_t)(v * 448 + (lane >> 3)) * 1024 + ct * 128 + (lane & 7) * 16;
      const unsigned ao = WS_A + ((qb * 8 + u * 2) * KSTEPS + 32 + v * 28) * 1024;
      u32x4 wr[PD][2], ar[PD][2];
#define FETCH2(i, slot) { wr[slot][0] = *(const u32x4*)(wsrc + (i) * 16384); wr[slot][1] = *(const u32x4*)(wsrc + (i) * 16384 + 8192); \
        ar[slot][0] = __builtin_amdgcn_raw_buffer_load_b128(rs, lane * 16, ao + (i) * 1024, SC1);                                        \
        ar[slot][1] = __builtin_amdgcn_raw_buffer_load_b128(rs, lane * 16, ao + (KSTEPS + (i)) * 1024, SC1); }
#pragma unroll
      for (int i = 0; i < PD; ++i) FETCH2(i, i)
#pragma unroll
      for (int i = 0; i < 4; ++i) acc[i] = f32x16{};
      const int wrow = lane >> 3;
      const lds_ptr wd = L + L_SCR + v * 4096 + wrow * 128 + (((lane & 7) ^ (((wrow >> 1) & 1) << 2)) << 4);
      const lds_cptr rsrc = (lds_cptr)L + L_SCR + v * 4096 + (4 * hh + q4) * 128 + g1 * 32 + p4 * 8;
#define PACE() { SBAR(); __builtin_amdgcn_s_sleep(1); SBAR(); }
#pragma unroll 1
      for (int i0 = 0; i0 < NS; i0 += PD) {
#pragma unroll
        for (int j = 0; j < PD; ++j) {
          const int buf = (j & 1) * 2048;
          *(LAS u32x4*)(wd + buf) = wr[j][0];
          *(LAS u32x4*)(wd + buf + 1024) = wr[j][1];
          const bf16x8 wa = ldtr2(rsrc + buf + w1x, rsrc + buf + 1024 + w1x);
          const bf16x8 wb = ldtr2(rsrc + buf + (w1x ^ 64), rsrc + buf + 1024 + (w1x ^ 64));
          const bf16x8 a0 = __builtin_bit_cast(bf16x8, ar[j][0]), a1 = __builtin_bit_cast(bf16x8, ar[j][1]);
          acc[0] = MFMA(wa, a0, acc[0]); PACE()
          acc[1] = MFMA(wa, a1, acc[1]); PACE()
          acc[2] = MFMA(wb, a0, acc[2]); PACE()
          acc[3] = MFMA(wb, a1, acc[3]);
          SBAR();
          const int in = i0 + PD + j < NS ? i0 + PD + j : NS - 1;
          FETCH2(in, j)
          SBAR();
        }
      }
    }
    STAMP(6);
    BAR();
#pragma unroll
    for (int a = 0; a < 4; ++a)
#pragma unroll
      for (int rq = 0; rq < 4; ++rq) {
        const f32x4 t = {acc[a][4 * rq], acc[a][4 * rq + 1], acc[a][4 * rq + 2], acc[a][4 * rq + 3]};
        *(LAS f32x4*)(L + F_AUXP + v * 16384 + (a * 4 + rq) * 1024 + lane * 16) = t;
      }
    BAR();
    STAMP(7);
    STAMP_OUT;
  }
}

extern "C" void kernel_launch(void* const* d_in, const int* in_sizes, int n_in, void* d_out, int out_size,
                              void* d_ws, size_t ws_size, hipStream_t stream) {
  (void)hipFuncSetAttribute((const void*)fused_k, hipFuncAttributeMaxDynamicSharedMemorySize, LDS_BYTES);
  fused_k<<<256, 768, LDS_BYTES, stream>>>((const char*)d_in[0], (const char*)d_in[1], (const char*)d_in[2],
                                           (const char*)d_in[4], (const char*)d_in[5], (const char*)d_in[6],
                                           (const char*)d_in[7], (const char*)d_in[8], (char*)d_ws, (char*)d_out);
}
```

```cpp
#include <hip/hip_runtime.h>
#include <hip/hip_bf16.h>
#include <stdint.h>

typedef __attribute__((ext_vector_type(8))) __bf16 bf16x8;
typedef __attribute__((ext_vector_type(2))) __bf16 bf16x2;
typedef __attribute__((ext_vector_type(4))) short s16x4;
typedef __attribute__((ext_vector_type(8))) short s16x8;
typedef __attribute__((ext_vector_type(16))) float f32x16;
typedef __attribute__((ext_vector_type(4))) float f32x4;
typedef __attribute__((ext_vector_type(4))) unsigned u32x4;
typedef __attribute__((ext_vector_type(2))) unsigned u32x2;
typedef unsigned long long u64;
#define LAS __attribute__((address_space(3)))
#define GAS __attribute__((address_space(1)))
typedef LAS const char* lds_cptr;
typedef LAS char* lds_ptr;
typedef GAS u64 gu64;

#define NO_FFN2 0
#define APRIO 0
#define PSLEEP 0
#define ROT2 1
#define ABL 0
#define GA 3
#define LDELAY 6
#define QPRIO 2
constexpr int FF = 1792;
constexpr int KSTEPS = 32 + FF / 16;

constexpr unsigned WS_E = 0;
constexpr unsigned WS_FKV = 4096;
constexpr unsigned WS_FH = WS_FKV + 1024 * 8;
constexpr unsigned WS_G = WS_FH + 1024 * 8;
constexpr unsigned WS_KV = 1u << 20;
constexpr unsigned WS_A = WS_KV + 32u * 64 * 2048;
constexpr unsigned WS_END = WS_A + 64u * KSTEPS * 1024;

constexpr unsigned OOB = 0x40000000u;

constexpr int ST_QK = 32768, ST_V = ST_QK + 4096, ST_W1 = ST_V + 2048, ST_BYTES = ST_W1 + 8192;
constexpr int L_SCR = 131072;
constexpr int L_GATE = L_SCR + 16384, L_QX = L_GATE + 1024, L_G = L_QX + 4096, L_E = L_G + 1024, L_CNT = L_E + 16;
constexpr int C_DONE = 0, C_HALF = 1, C_LATE = 2, C_X0 = 3, C_X1 = 4, C_FIN = 5, C_READY = 16;
constexpr int L_ZERO = L_CNT + 28 * 4;
constexpr int LDS_BYTES = L_CNT + 128;

#define STAMP(i)
#define STAMP_DECL
#define STAMP_OUT
#define MFMA(a, b, c) __builtin_amdgcn_mfma_f32_32x32x16_bf16(a, b, c, 0, 0, 0)
#define MFMA16(a, b, c) __builtin_amdgcn_mfma_f32_16x16x32_bf16(a, b, c, 0, 0, 0)
#define INTERLEAVE(n) { _Pragma("unroll") for (int i_ = 0; i_ < (n); ++i_) { __builtin_amdgcn_sched_group_barrier(0x008, 1, 0); __builtin_amdgcn_sched_group_barrier(0x100, 1, 0); } __builtin_amdgcn_sched_group_barrier(0x008, 16, 0); }
#define BAR() asm volatile("s_waitcnt lgkmcnt(0)\n\ts_barrier" ::: "memory")
#define XCH(w) (((w) < 2 ? L_SCR : 49152) + (w) * 8192)
#define LANE_VARS int lane; asm volatile("v_mbcnt_lo_u32_b32 %0, -1, 0\n\tv_mbcnt_hi_u32_b32 %0, -1, %0" : "=v"(lane)); __builtin_assume(lane >= 0 && lane < 64);                       \
  const int r32 = lane & 31, hh = lane >> 5, g1 = (lane >> 4) & 1, g4 = lane >> 4, r16 = lane & 15, q4 = (lane >> 2) & 3, p4 = lane & 3; \
  (void)r32; (void)hh; (void)g1; (void)g4; (void)r16; (void)q4; (void)p4;
#define DMA_VARS                                                                                                 \
  const unsigned xv0 = (lane >> 3) * 1024 + (((lane & 7) ^ g4) << 4), xv1 = xv0 ^ 64;                            \
  const unsigned qv_ = (lane >> 2) * 3072 + (((p4 >> 1) ^ hh) << 10) + (p4 & 1) * 16;                            \
  const unsigned vv_ = ((r32 >> 1) ^ (4 * g1)) * 3072 + 2048 + (lane & 1) * 16;                                  \
  const unsigned wv0 = (lane >> 3) * (FF * 2) + (((lane & 7) ^ (2 * g1)) << 4), wv1 = wv0 ^ 64;                  \
  (void)qv_; (void)vv_; (void)wv0; (void)wv1;
#define SBAR() __builtin_amdgcn_sched_barrier(0)
#define PIN(x) asm volatile("" : "+v"(x))
#define RLX_AGENT __ATOMIC_RELAXED, __HIP_MEMORY_SCOPE_AGENT
#define SC1 16

__device__ __forceinline__ unsigned cvtpk(float lo, float hi) {
  bf16x2 v; v[0] = (__bf16)lo; v[1] = (__bf16)hi; return __builtin_bit_cast(unsigned, v);
}
__device__ __forceinline__ float bfround(float x) { return (float)(__bf16)x; }
__device__ __forceinline__ bf16x8 ldtr2(lds_cptr p0, lds_cptr p1) {
  s16x4 a = __builtin_amdgcn_ds_read_tr16_b64_v4i16((LAS s16x4*)p0), b = __builtin_amdgcn_ds_read_tr16_b64_v4i16((LAS s16x4*)p1);
  s16x8 r = {a[0], a[1], a[2], a[3], b[0], b[1], b[2], b[3]};
  return __builtin_bit_cast(bf16x8, r);
}
__device__ __forceinline__ bf16x8 ld128(lds_cptr p) { return *(const LAS bf16x8*)p; }
__device__ __forceinline__ void glds16(const char* sbase, unsigned voff, unsigned lds_base) {
  unsigned sv;
  asm volatile("s_mov_b32 %0, m0\n\ts_mov_b32 m0, %3\n\ts_nop 0\n\tglobal_load_lds_dwordx4 %1, %2\n\ts_mov_b32 m0, %0"
               : "=&s"(sv) : "v"(voff), "s"(sbase), "s"(__builtin_amdgcn_readfirstlane(lds_base)) : "memory");
}
__device__ __forceinline__ void st_sc1(u32x4 v, __amdgpu_buffer_rsrc_t rs, unsigned off) { __builtin_amdgcn_raw_buffer_store_b128(v, rs, off, 0, SC1); }
__device__ __forceinline__ float swap_max(float v) {
  auto rr = __builtin_amdgcn_permlane32_swap(__float_as_uint(v), __float_as_uint(v), false, false);
  return fmaxf(__uint_as_float(rr[0]), __uint_as_float(rr[1]));
}
__device__ __forceinline__ u32x4 merge16(f32x4 a, f32x4 b, float s) {
  auto r0 = __builtin_amdgcn_permlane16_swap(cvtpk(a[0] * s, a[1] * s), cvtpk(b[0] * s, b[1] * s), false, false);
  auto r1 = __builtin_amdgcn_permlane16_swap(cvtpk(a[2] * s, a[3] * s), cvtpk(b[2] * s, b[3] * s), false, false);
  return u32x4{r0[0], r1[0], r0[1], r1[1]};
}
__device__ __forceinline__ u32x4 merge32(f32x4 a, f32x4 b) {
  auto r0 = __builtin_amdgcn_permlane32_swap(cvtpk(a[0], a[1]), cvtpk(b[0], b[1]), false, false);
  auto r1 = __builtin_amdgcn_permlane32_swap(cvtpk(a[2], a[3]), cvtpk(b[2], b[3]), false, false);
  return u32x4{r0[0], r1[0], r0[1], r1[1]};
}
__device__ __forceinline__ u32x4 not_empty(u32x4 v) { return u32x4{min(v[0], 0xfffffffeu), min(v[1], 0xfffffffeu), min(v[2], 0xfffffffeu), min(v[3], 0xfffffffeu)}; }
__device__ __forceinline__ unsigned umax4(u32x4 v) { return max(max(v[0], v[1]), max(v[2], v[3])); }
__device__ __forceinline__ u32x4 ld_sc1(__amdgpu_buffer_rsrc_t rs, unsigned voff, unsigned soff) { return __builtin_amdgcn_raw_buffer_load_b128(rs, voff, soff, SC1); }
__device__ __forceinline__ float gate_from_logit(float z) {
  z = bfround(z);
  return bfround(1.0f / (1.0f + __expf(-z)));
}
__device__ __forceinline__ float max16(const f32x16& s) {
  float a = fmaxf(fmaxf(s[0], s[1]), s[2]);
#pragma unroll
  for (int r = 3; r < 15; r += 2) a = fmaxf(fmaxf(a, s[r]), s[r + 1]);
  return fmaxf(a, s[15]);
}
__device__ __forceinline__ void pack_p(const f32x16& s, bf16x8& p0, bf16x8& p1) {
  u32x4 a, b;
#pragma unroll
  for (int i = 0; i < 4; ++i) { a[i] = cvtpk(s[2 * i], s[2 * i + 1]); b[i] = cvtpk(s[8 + 2 * i], s[8 + 2 * i + 1]); }
  p0 = __builtin_bit_cast(bf16x8, a); p1 = __builtin_bit_cast(bf16x8, b);
}
__device__ __forceinline__ u64 make_tag(u64 e) {
  const unsigned lo = __builtin_amdgcn_readfirstlane((unsigned)e), hi = __builtin_amdgcn_readfirstlane((unsigned)(e >> 32));
  return (((u64)hi << 32) | lo) * 0x9E3779B97F4A7C15ull + 0xD1B54A32D192ED03ull;
}
__device__ __forceinline__ void lds_arrive(unsigned addr, unsigned n, int lane) {
  asm volatile("s_waitcnt lgkmcnt(0)" ::: "memory");
  if (lane == 0) asm volatile("ds_add_u32 %0, %1" :: "v"(addr), "v"(n) : "memory");
}
constexpr unsigned SPIN_CAP = 1u << 20;
__device__ __forceinline__ void lds_wait(lds_cptr p, unsigned want) {
#pragma unroll 1
  for (unsigned spins = 0; spins < SPIN_CAP; ++spins) {
    const unsigned v = __builtin_amdgcn_readfirstlane(*(const volatile LAS unsigned*)p);
    if (v >= want) break;
    __builtin_amdgcn_s_sleep(1);
  }
  asm volatile("" ::: "memory");
}
__device__ __forceinline__ void lds_wait_slow(lds_cptr p, unsigned want) {
#pragma unroll 1
  for (unsigned spins = 0; spins < (SPIN_CAP >> 3); ++spins) {
    const unsigned v = __builtin_amdgcn_readfirstlane(*(const volatile LAS unsigned*)p);
    if (v >= want) break;
    __builtin_amdgcn_s_sleep(12);
  }
  asm volatile("" ::: "memory");
}
__device__ __forceinline__ void flags_wait(const char* ws, unsigned off, u64 tag) {
  gu64* f = (gu64*)(ws + off);
#pragma unroll 1
  for (unsigned spins = 0; spins < (SPIN_CAP >> 4); ++spins) {
    const bool ok = __hip_atomic_load(f, RLX_AGENT) == tag;
    if (__all(ok)) break;
    __builtin_amdgcn_s_sleep(1);
  }
  asm volatile("s_waitcnt vmcnt(0)" ::: "memory");
}
__device__ __forceinline__ void flag_set(char* ws, unsigned off, u64 tag, int lane) {
  asm volatile("s_waitcnt vmcnt(0)" ::: "memory");
  if (lane == 0) __hip_atomic_store((gu64*)(ws + off), tag, RLX_AGENT);
}

#define ATT_ASM \
  "s_nop 15\n" \
  "Latt_loop%=:\n" \
  "s_waitcnt lgkmcnt(2)\n" \
  "v_mfma_f32_32x32x16_bf16 v[80:95], v[132:135], v[128:131], v[96:111]\n" \
  "ds_read_b128 v[132:135], v152 offset:4096\n" \
  "v_exp_f32 v64, v64\n" \
  "v_exp_f32 v65, v65\n" \
  "v_exp_f32 v66, v66\n" \
  "v_exp_f32 v67, v67\n" \
  "v_exp_f32 v68, v68\n" \
  "s_waitcnt lgkmcnt(1)\n" \
  "v_mfma_f32_32x32x16_bf16 v[112:127], v[136:139], v[144:147], v[112:127]\n" \
  "v_exp_f32 v69, v69\n" \
  "v_exp_f32 v70, v70\n" \
  "v_exp_f32 v71, v71\n" \
  "v_exp_f32 v72, v72\n" \
  "v_exp_f32 v73, v73\n" \
  "v_mfma_f32_32x32x16_bf16 v[112:127], v[140:143], v[148:151], v[112:127]\n" \
  "s_mov_b64 exec, %[msk]\n" \
  "ds_read_b128 v[136:139], v153 offset:0\n" \
  "ds_read_b128 v[140:143], v153 offset:512\n" \
  "s_mov_b64 exec, -1\n" \
  "v_exp_f32 v74, v74\n" \
  "v_exp_f32 v75, v75\n" \
  "v_exp_f32 v76, v76\n" \
  "v_exp_f32 v77, v77\n" \
  "v_exp_f32 v78, v78\n" \
  "v_exp_f32 v79, v79\n" \
  "v_cvt_pk_bf16_f32 v144, v64, v65\n" \
  "v_cvt_pk_bf16_f32 v145, v66, v67\n" \
  "v_cvt_pk_bf16_f32 v146, v68, v69\n" \
  "v_cvt_pk_bf16_f32 v147, v70, v71\n" \
  "v_cvt_pk_bf16_f32 v148, v72, v73\n" \
  "v_cvt_pk_bf16_f32 v149, v74, v75\n" \
  "v_cvt_pk_bf16_f32 v150, v76, v77\n" \
  "v_cvt_pk_bf16_f32 v151, v78, v79\n" \
  "s_waitcnt lgkmcnt(2)\n" \
  "v_mfma_f32_32x32x16_bf16 v[64:79], v[132:135], v[128:131], v[96:111]\n" \
  "ds_read_b128 v[132:135], v152 offset:6144\n" \
  "v_exp_f32 v80, v80\n" \
  "v_exp_f32 v81, v81\n" \
  "v_exp_f32 v82, v82\n" \
  "v_exp_f32 v83, v83\n" \
  "v_exp_f32 v84, v84\n" \
  "s_waitcnt lgkmcnt(1)\n" \
  "v_mfma_f32_32x32x16_bf16 v[112:127], v[136:139], v[144:147], v[112:127]\n" \
  "v_exp_f32 v85, v85\n" \
  "v_exp_f32 v86, v86\n" \
  "v_exp_f32 v87, v87\n" \
  "v_exp_f32 v88, v88\n" \
  "v_exp_f32 v89, v89\n" \
  "v_mfma_f32_32x32x16_bf16 v[112:127], v[140:143], v[148:151], v[112:127]\n" \
  "s_mov_b64 exec, %[msk]\n" \
  "ds_read_b128 v[136:139], v153 offset:2048\n" \
  "ds_read_b128 v[140:143], v153 offset:2560\n" \
  "s_mov_b64 exec, -1\n" \
  "v_exp_f32 v90, v90\n" \
  "v_exp_f32 v91, v91\n" \
  "v_exp_f32 v92, v92\n" \
  "v_exp_f32 v93, v93\n" \
  "v_exp_f32 v94, v94\n" \
  "v_exp_f32 v95, v95\n" \
  "v_cvt_pk_bf16_f32 v144, v80, v81\n" \
  "v_cvt_pk_bf16_f32 v145, v82, v83\n" \
  "v_cvt_pk_bf16_f32 v146, v84, v85\n" \
  "v_cvt_pk_bf16_f32 v147, v86, v87\n" \
  "v_cvt_pk_bf16_f32 v148, v88, v89\n" \
  "v_cvt_pk_bf16_f32 v149, v90, v91\n" \
  "v_cvt_pk_bf16_f32 v150, v92, v93\n" \
  "v_cvt_pk_bf16_f32 v151, v94, v95\n" \
  "s_waitcnt lgkmcnt(2)\n" \
  "v_mfma_f32_32x32x16_bf16 v[80:95], v[132:135], v[128:131], v[96:111]\n" \
  "ds_read_b128 v[132:135], v152 offset:8192\n" \
  "v_exp_f32 v64, v64\n" \
  "v_exp_f32 v65, v65\n" \
  "v_exp_f32 v66, v66\n" \
  "v_exp_f32 v67, v67\n" \
  "v_exp_f32 v68, v68\n" \
  "s_waitcnt lgkmcnt(1)\n" \
  "v_mfma_f32_32x32x16_bf16 v[112:127], v[136:139], v[144:147], v[112:127]\n" \
  "v_exp_f32 v69, v69\n" \
  "v_exp_f32 v70, v70\n" \
  "v_exp_f32 v71, v71\n" \
  "v_exp_f32 v72, v72\n" \
  "v_exp_f32 v73, v73\n" \
  "v_mfma_f32_32x32x16_bf16 v[112:127], v[140:143], v[148:151], v[112:127]\n" \
  "s_mov_b64 exec, %[msk]\n" \
  "ds_read_b128 v[136:139], v153 offset:4096\n" \
  "ds_read_b128 v[140:143], v153 offset:4608\n" \
  "s_mov_b64 exec, -1\n" \
  "v_exp_f32 v74, v74\n" \
  "v_exp_f32 v75, v75\n" \
  "v_exp_f32 v76, v76\n" \
  "v_exp_f32 v77, v77\n" \
  "v_exp_f32 v78, v78\n" \
  "v_exp_f32 v79, v79\n" \
  "v_cvt_pk_bf16_f32 v144, v64, v65\n" \
  "v_cvt_pk_bf16_f32 v145, v66, v67\n" \
  "v_cvt_pk_bf16_f32 v146, v68, v69\n" \
  "v_cvt_pk_bf16_f32 v147, v70, v71\n" \
  "v_cvt_pk_bf16_f32 v148, v72, v73\n" \
  "v_cvt_pk_bf16_f32 v149, v74, v75\n" \
  "v_cvt_pk_bf16_f32 v150, v76, v77\n" \
  "v_cvt_pk_bf16_f32 v151, v78, v79\n" \
  "s_waitcnt lgkmcnt(2)\n" \
  "v_mfma_f32_32x32x16_bf16 v[64:79], v[132:135], v[128:131], v[96:111]\n" \
  "ds_read_b128 v[132:135], v152 offset:10240\n" \
  "v_exp_f32 v80, v80\n" \
  "v_exp_f32 v81, v81\n" \
  "v_exp_f32 v82, v82\n" \
  "v_exp_f32 v83, v83\n" \
  "v_exp_f32 v84, v84\n" \
  "s_waitcnt lgkmcnt(1)\n" \
  "v_mfma_f32_32x32x16_bf16 v[112:127], v[136:139], v[144:147], v[112:127]\n" \
  "v_exp_f32 v85, v85\n" \
  "v_exp_f32 v86, v86\n" \
  "v_exp_f32 v87, v87\n" \
  "v_exp_f32 v88, v88\n" \
  "v_exp_f32 v89, v89\n" \
  "v_mfma_f32_32x32x16_bf16 v[112:127], v[140:143], v[148:151], v[112:127]\n" \
  "s_mov_b64 exec, %[msk]\n" \
  "ds_read_b128 v[136:139], v153 offset:6144\n" \
  "ds_read_b128 v[140:143], v153 offset:6656\n" \
  "s_mov_b64 exec, -1\n" \
  "v_exp_f32 v90, v90\n" \
  "v_exp_f32 v91, v91\n" \
  "v_exp_f32 v92, v92\n" \
  "v_exp_f32 v93, v93\n" \
  "v_exp_f32 v94, v94\n" \
  "v_exp_f32 v95, v95\n" \
  "v_cvt_pk_bf16_f32 v144, v80, v81\n" \
  "v_cvt_pk_bf16_f32 v145, v82, v83\n" \
  "v_cvt_pk_bf16_f32 v146, v84, v85\n" \
  "v_cvt_pk_bf16_f32 v147, v86, v87\n" \
  "v_cvt_pk_bf16_f32 v148, v88, v89\n" \
  "v_cvt_pk_bf16_f32 v149, v90, v91\n" \
  "v_cvt_pk_bf16_f32 v150, v92, v93\n" \
  "v_cvt_pk_bf16_f32 v151, v94, v95\n" \
  "s_waitcnt lgkmcnt(2)\n" \
  "v_mfma_f32_32x32x16_bf16 v[80:95], v[132:135], v[128:131], v[96:111]\n" \
  "ds_read_b128 v[132:135], v152 offset:12288\n" \
  "v_exp_f32 v64, v64\n" \
  "v_exp_f32 v65, v65\n" \
  "v_exp_f32 v66, v66\n" \
  "v_exp_f32 v67, v67\n" \
  "v_exp_f32 v68, v68\n" \
  "s_waitcnt lgkmcnt(1)\n" \
  "v_mfma_f32_32x32x16_bf16 v[112:127], v[136:139], v[144:147], v[112:127]\n" \
  "ds_read_b32 v155, v154\n" \
  "v_exp_f32 v69, v69\n" \
  "v_exp_f32 v70, v70\n" \
  "v_exp_f32 v71, v71\n" \
  "v_exp_f32 v72, v72\n" \
  "v_exp_f32 v73, v73\n" \
  "v_mfma_f32_32x32x16_bf16 v[112:127], v[140:143], v[148:151], v[112:127]\n" \
  "s_mov_b64 exec, %[msk]\n" \
  "ds_read_b128 v[136:139], v153 offset:8192\n" \
  "ds_read_b128 v[140:143], v153 offset:8704\n" \
  "s_mov_b64 exec, -1\n" \
  "v_exp_f32 v74, v74\n" \
  "v_exp_f32 v75, v75\n" \
  "v_exp_f32 v76, v76\n" \
  "v_exp_f32 v77, v77\n" \
  "v_exp_f32 v78, v78\n" \
  "v_exp_f32 v79, v79\n" \
  "v_cvt_pk_bf16_f32 v144, v64, v65\n" \
  "v_cvt_pk_bf16_f32 v145, v66, v67\n" \
  "v_cvt_pk_bf16_f32 v146, v68, v69\n" \
  "v_cvt_pk_bf16_f32 v147, v70, v71\n" \
  "v_cvt_pk_bf16_f32 v148, v72, v73\n" \
  "v_cvt_pk_bf16_f32 v149, v74, v75\n" \
  "v_cvt_pk_bf16_f32 v150, v76, v77\n" \
  "v_cvt_pk_bf16_f32 v151, v78, v79\n" \
  "s_waitcnt lgkmcnt(2)\n" \
  "v_mfma_f32_32x32x16_bf16 v[64:79], v[132:135], v[128:131], v[96:111]\n" \
  "ds_read_b128 v[132:135], v152 offset:14336\n" \
  "v_exp_f32 v80, v80\n" \
  "v_exp_f32 v81, v81\n" \
  "v_exp_f32 v82, v82\n" \
  "v_exp_f32 v83, v83\n" \
  "v_exp_f32 v84, v84\n" \
  "s_waitcnt lgkmcnt(1)\n" \
  "v_mfma_f32_32x32x16_bf16 v[112:127], v[136:139], v[144:147], v[112:127]\n" \
  "v_exp_f32 v85, v85\n" \
  "v_exp_f32 v86, v86\n" \
  "v_exp_f32 v87, v87\n" \
  "v_exp_f32 v88, v88\n" \
  "v_exp_f32 v89, v89\n" \
  "v_mfma_f32_32x32x16_bf16 v[112:127], v[140:143], v[148:151], v[112:127]\n" \
  "s_mov_b64 exec, %[msk]\n" \
  "ds_read_b128 v[136:139], v153 offset:10240\n" \
  "ds_read_b128 v[140:143], v153 offset:10752\n" \
  "s_mov_b64 exec, -1\n" \
  "v_exp_f32 v90, v90\n" \
  "v_exp_f32 v91, v91\n" \
  "v_exp_f32 v92, v92\n" \
  "v_exp_f32 v93, v93\n" \
  "v_exp_f32 v94, v94\n" \
  "v_exp_f32 v95, v95\n" \
  "v_cvt_pk_bf16_f32 v144, v80, v81\n" \
  "v_cvt_pk_bf16_f32 v145, v82, v83\n" \
  "v_cvt_pk_bf16_f32 v146, v84, v85\n" \
  "v_cvt_pk_bf16_f32 v147, v86, v87\n" \
  "v_cvt_pk_bf16_f32 v148, v88, v89\n" \
  "v_cvt_pk_bf16_f32 v149, v90, v91\n" \
  "v_cvt_pk_bf16_f32 v150, v92, v93\n" \
  "v_cvt_pk_bf16_f32 v151, v94, v95\n" \
  "v_readfirstlane_b32 %[tmp], v155\n" \
  "s_cmp_ge_u32 %[tmp], 8\n" \
  "s_cbranch_scc1 Latt_ok%=\n" \
  "s_mov_b32 %[spin], 0x100000\n" \
  "Latt_spin%=:\n" \
  "s_sleep 1\n" \
  "ds_read_b32 v155, v154\n" \
  "s_waitcnt lgkmcnt(0)\n" \
  "v_readfirstlane_b32 %[tmp], v155\n" \
  "s_sub_u32 %[spin], %[spin], 1\n" \
  "s_cmp_eq_u32 %[spin], 0\n" \
  "s_cbranch_scc1 Latt_ok%=\n" \
  "s_cmp_lt_u32 %[tmp], 8\n" \
  "s_cbranch_scc1 Latt_spin%=\n" \
  "Latt_ok%=:\n" \
  "s_waitcnt lgkmcnt(2)\n" \
  "v_mfma_f32_32x32x16_bf16 v[80:95], v[132:135], v[128:131], v[96:111]\n" \
  "ds_read_b128 v[132:135], v152 offset:16384\n" \
  "v_exp_f32 v64, v64\n" \
  "v_exp_f32 v65, v65\n" \
  "v_exp_f32 v66, v66\n" \
  "v_exp_f32 v67, v67\n" \
  "v_exp_f32 v68, v68\n" \
  "s_waitcnt lgkmcnt(1)\n" \
  "v_mfma_f32_32x32x16_bf16 v[112:127], v[136:139], v[144:147], v[112:127]\n" \
  "v_exp_f32 v69, v69\n" \
  "v_exp_f32 v70, v70\n" \
  "v_exp_f32 v71, v71\n" \
  "v_exp_f32 v72, v72\n" \
  "v_exp_f32 v73, v73\n" \
  "v_mfma_f32_32x32x16_bf16 v[112:127], v[140:143], v[148:151], v[112:127]\n" \
  "s_mov_b64 exec, %[msk]\n" \
  "ds_read_b128 v[136:139], v153 offset:12288\n" \
  "ds_read_b128 v[140:143], v153 offset:12800\n" \
  "s_mov_b64 exec, -1\n" \
  "v_exp_f32 v74, v74\n" \
  "v_exp_f32 v75, v75\n" \
  "v_exp_f32 v76, v76\n" \
  "v_exp_f32 v77, v77\n" \
  "v_exp_f32 v78, v78\n" \
  "v_exp_f32 v79, v79\n" \
  "v_cvt_pk_bf16_f32 v144, v64, v65\n" \
  "v_cvt_pk_bf16_f32 v145, v66, v67\n" \
  "v_cvt_pk_bf16_f32 v146, v68, v69\n" \
  "v_cvt_pk_bf16_f32 v147, v70, v71\n" \
  "v_cvt_pk_bf16_f32 v148, v72, v73\n" \
  "v_cvt_pk_bf16_f32 v149, v74, v75\n" \
  "v_cvt_pk_bf16_f32 v150, v76, v77\n" \
  "v_cvt_pk_bf16_f32 v151, v78, v79\n" \
  "s_waitcnt lgkmcnt(2)\n" \
  "v_mfma_f32_32x32x16_bf16 v[64:79], v[132:135], v[128:131], v[96:111]\n" \
  "ds_read_b128 v[132:135], v152 offset:18432\n" \
  "v_exp_f32 v80, v80\n" \
  "v_exp_f32 v81, v81\n" \
  "v_exp_f32 v82, v82\n" \
  "v_exp_f32 v83, v83\n" \
  "v_exp_f32 v84, v84\n" \
  "s_waitcnt lgkmcnt(1)\n" \
  "v_mfma_f32_32x32x16_bf16 v[112:127], v[136:139], v[144:147], v[112:127]\n" \
  "v_exp_f32 v85, v85\n" \
  "v_exp_f32 v86, v86\n" \
  "v_exp_f32 v87, v87\n" \
  "v_exp_f32 v88, v88\n" \
  "v_exp_f32 v89, v89\n" \
  "v_mfma_f32_32x32x16_bf16 v[112:127], v[140:143], v[148:151], v[112:127]\n" \
  "s_mov_b64 exec, %[msk]\n" \
  "ds_read_b128 v[136:139], v153 offset:14336\n" \
  "ds_read_b128 v[140:143], v153 offset:14848\n" \
  "s_mov_b64 exec, -1\n" \
  "v_exp_f32 v90, v90\n" \
  "v_exp_f32 v91, v91\n" \
  "v_exp_f32 v92, v92\n" \
  "v_exp_f32 v93, v93\n" \
  "v_exp_f32 v94, v94\n" \
  "v_exp_f32 v95, v95\n" \
  "v_cvt_pk_bf16_f32 v144, v80, v81\n" \
  "v_cvt_pk_bf16_f32 v145, v82, v83\n" \
  "v_cvt_pk_bf16_f32 v146, v84, v85\n" \
  "v_cvt_pk_bf16_f32 v147, v86, v87\n" \
  "v_cvt_pk_bf16_f32 v148, v88, v89\n" \
  "v_cvt_pk_bf16_f32 v149, v90, v91\n" \
  "v_cvt_pk_bf16_f32 v150, v92, v93\n" \
  "v_cvt_pk_bf16_f32 v151, v94, v95\n" \
  "v_add_u32 v152, 16384, v152\n" \
  "v_add_u32 v153, 16384, v153\n" \
  "v_add_u32 v154, 4, v154\n" \
  "s_sub_u32 %[n], %[n], 1\n" \
  "s_cmp_lg_u32 %[n], 0\n" \
  "s_cbranch_scc1 Latt_loop%=\n" \
  "s_waitcnt lgkmcnt(0)\n" \
  "s_nop 3\n"

#define TAIL_ASM \
  "ds_read_b64_tr_b16 v[128:129], %[la] offset:0\n" \
  "ds_read_b64_tr_b16 v[130:131], %[la] offset:1024\n" \
  "ds_read_b64_tr_b16 v[132:133], %[lb] offset:0\n" \
  "ds_read_b64_tr_b16 v[134:135], %[lb] offset:1024\n" \
  "ds_read_b64_tr_b16 v[136:137], %[la] offset:2048\n" \
  "ds_read_b64_tr_b16 v[138:139], %[la] offset:3072\n" \
  "ds_read_b64_tr_b16 v[140:141], %[lb] offset:2048\n" \
  "ds_read_b64_tr_b16 v[142:143], %[lb] offset:3072\n" \
  "ds_read_b64_tr_b16 v[144:145], %[la] offset:4096\n" \
  "ds_read_b64_tr_b16 v[146:147], %[la] offset:5120\n" \
  "ds_read_b64_tr_b16 v[148:149], %[lb] offset:4096\n" \
  "ds_read_b64_tr_b16 v[150:151], %[lb] offset:5120\n" \
  "ds_read_b64_tr_b16 v[152:153], %[la] offset:6144\n" \
  "ds_read_b64_tr_b16 v[154:155], %[la] offset:7168\n" \
  "ds_read_b64_tr_b16 v[156:157], %[lb] offset:6144\n" \
  "ds_read_b64_tr_b16 v[158:159], %[lb] offset:7168\n" \
  "v_add_u32 v161, 0x1000, %[vo]\n" \
  "v_add_u32 v162, 0x2000, %[vo]\n" \
  "v_add_u32 v163, 0x3000, %[vo]\n" \
  "v_mov_b32 v64, -1\n" \
  "v_mov_b32 v68, -1\n" \
  "v_mov_b32 v72, -1\n" \
  "v_mov_b32 v76, -1\n" \
  "v_mov_b32 v80, -1\n" \
  "v_mov_b32 v84, -1\n" \
  "v_mov_b32 v88, -1\n" \
  "v_mov_b32 v92, -1\n" \
  "v_mov_b32 v96, -1\n" \
  "v_mov_b32 v100, -1\n" \
  "v_mov_b32 v104, -1\n" \
  "v_mov_b32 v108, -1\n" \
  "v_mov_b32 v112, -1\n" \
  "v_mov_b32 v116, -1\n" \
  "v_mov_b32 v120, -1\n" \
  "v_mov_b32 v124, -1\n" \
  "s_mov_b32 %[todo], 0xffff\n" \
  "s_mov_b32 %[nx], 0\n" \
  "s_mov_b32 %[sp], 0x10000\n" \
  "Lt_loop%=:\n" \
  "s_mov_b32 %[m], %[todo]\n" \
  "s_bcnt1_i32_b32 %[t], %[todo]\n" \
  "s_cmp_le_u32 %[t], 6\n" \
  "s_cbranch_scc1 Lt_fetch%=\n" \
  "global_load_dword v160, %[vp], %[ob] sc1\n" \
  "s_waitcnt vmcnt(0)\n" \
  "v_cmp_ne_u32 vcc, -1, v160\n" \
  "s_nop 1\n" \
  "s_and_b32 %[m], %[m], vcc_lo\n" \
  "s_cbranch_scc1 Lt_fetch%=\n" \
  "s_sleep 2\n" \
  "s_branch Lt_next%=\n" \
  "Lt_fetch%=:\n" \
  "s_bitcmp1_b32 %[m], 0\n" \
  "s_cbranch_scc0 Lt_s0_%=\n" \
  "global_load_dwordx4 v[64:67], %[vo], %[ob] offset:0 sc1\n" \
  "Lt_s0_%=:\n" \
  "s_bitcmp1_b32 %[m], 1\n" \
  "s_cbranch_scc0 Lt_s1_%=\n" \
  "global_load_dwordx4 v[68:71], %[vo], %[ob] offset:1024 sc1\n" \
  "Lt_s1_%=:\n" \
  "s_bitcmp1_b32 %[m], 2\n" \
  "s_cbranch_scc0 Lt_s2_%=\n" \
  "global_load_dwordx4 v[72:75], %[vo], %[ob] offset:2048 sc1\n" \
  "Lt_s2_%=:\n" \
  "s_bitcmp1_b32 %[m], 3\n" \
  "s_cbranch_scc0 Lt_s3_%=\n" \
  "global_load_dwordx4 v[76:79], %[vo], %[ob] offset:3072 sc1\n" \
  "Lt_s3_%=:\n" \
  "s_bitcmp1_b32 %[m], 4\n" \
  "s_cbranch_scc0 Lt_s4_%=\n" \
  "global_load_dwordx4 v[80:83], v161, %[ob] offset:0 sc1\n" \
  "Lt_s4_%=:\n" \
  "s_bitcmp1_b32 %[m], 5\n" \
  "s_cbranch_scc0 Lt_s5_%=\n" \
  "global_load_dwordx4 v[84:87], v161, %[ob] offset:1024 sc1\n" \
  "Lt_s5_%=:\n" \
  "s_bitcmp1_b32 %[m], 6\n" \
  "s_cbranch_scc0 Lt_s6_%=\n" \
  "global_load_dwordx4 v[88:91], v161, %[ob] offset:2048 sc1\n" \
  "Lt_s6_%=:\n" \
  "s_bitcmp1_b32 %[m], 7\n" \
  "s_cbranch_scc0 Lt_s7_%=\n" \
  "global_load_dwordx4 v[92:95], v161, %[ob] offset:3072 sc1\n" \
  "Lt_s7_%=:\n" \
  "s_bitcmp1_b32 %[m], 8\n" \
  "s_cbranch_scc0 Lt_s8_%=\n" \
  "global_load_dwordx4 v[96:99], v162, %[ob] offset:0 sc1\n" \
  "Lt_s8_%=:\n" \
  "s_bitcmp1_b32 %[m], 9\n" \
  "s_cbranch_scc0 Lt_s9_%=\n" \
  "global_load_dwordx4 v[100:103], v162, %[ob] offset:1024 sc1\n" \
  "Lt_s9_%=:\n" \
  "s_bitcmp1_b32 %[m], 10\n" \
  "s_cbranch_scc0 Lt_s10_%=\n" \
  "global_load_dwordx4 v[104:107], v162, %[ob] offset:2048 sc1\n" \
  "Lt_s10_%=:\n" \
  "s_bitcmp1_b32 %[m], 11\n" \
  "s_cbranch_scc0 Lt_s11_%=\n" \
  "global_load_dwordx4 v[108:111], v162, %[ob] offset:3072 sc1\n" \
  "Lt_s11_%=:\n" \
  "s_bitcmp1_b32 %[m], 12\n" \
  "s_cbranch_scc0 Lt_s12_%=\n" \
  "global_load_dwordx4 v[112:115], v163, %[ob] offset:0 sc1\n" \
  "Lt_s12_%=:\n" \
  "s_bitcmp1_b32 %[m], 13\n" \
  "s_cbranch_scc0 Lt_s13_%=\n" \
  "global_load_dwordx4 v[116:119], v163, %[ob] offset:1024 sc1\n" \
  "Lt_s13_%=:\n" \
  "s_bitcmp1_b32 %[m], 14\n" \
  "s_cbranch_scc0 Lt_s14_%=\n" \
  "global_load_dwordx4 v[120:123], v163, %[ob] offset:2048 sc1\n" \
  "Lt_s14_%=:\n" \
  "s_bitcmp1_b32 %[m], 15\n" \
  "s_cbranch_scc0 Lt_s15_%=\n" \
  "global_load_dwordx4 v[124:127], v163, %[ob] offset:3072 sc1\n" \
  "Lt_s15_%=:\n" \
  "s_waitcnt vmcnt(0)\n" \
  "v_max3_u32 v164, v64, v65, v66\n" \
  "v_max_u32 v164, v164, v67\n" \
  "v_cmp_eq_u32 s[36:37], -1, v164\n" \
  "v_max3_u32 v165, v68, v69, v70\n" \
  "v_max_u32 v165, v165, v71\n" \
  "v_cmp_eq_u32 s[38:39], -1, v165\n" \
  "v_max3_u32 v164, v72, v73, v74\n" \
  "v_max_u32 v164, v164, v75\n" \
  "v_cmp_eq_u32 s[40:41], -1, v164\n" \
  "v_max3_u32 v165, v76, v77, v78\n" \
  "v_max_u32 v165, v165, v79\n" \
  "v_cmp_eq_u32 s[42:43], -1, v165\n" \
  "v_max3_u32 v164, v80, v81, v82\n" \
  "v_max_u32 v164, v164, v83\n" \
  "v_cmp_eq_u32 s[44:45], -1, v164\n" \
  "v_max3_u32 v165, v84, v85, v86\n" \
  "v_max_u32 v165, v165, v87\n" \
  "v_cmp_eq_u32 s[46:47], -1, v165\n" \
  "v_max3_u32 v164, v88, v89, v90\n" \
  "v_max_u32 v164, v164, v91\n" \
  "v_cmp_eq_u32 s[48:49], -1, v164\n" \
  "v_max3_u32 v165, v92, v93, v94\n" \
  "v_max_u32 v165, v165, v95\n" \
  "v_cmp_eq_u32 s[50:51], -1, v165\n" \
  "v_max3_u32 v164, v96, v97, v98\n" \
  "v_max_u32 v164, v164, v99\n" \
  "v_cmp_eq_u32 s[52:53], -1, v164\n" \
  "v_max3_u32 v165, v100, v101, v102\n" \
  "v_max_u32 v165, v165, v103\n" \
  "v_cmp_eq_u32 s[54:55], -1, v165\n" \
  "v_max3_u32 v164, v104, v105, v106\n" \
  "v_max_u32 v164, v164, v107\n" \
  "v_cmp_eq_u32 s[56:57], -1, v164\n" \
  "v_max3_u32 v165, v108, v109, v110\n" \
  "v_max_u32 v165, v165, v111\n" \
  "v_cmp_eq_u32 s[58:59], -1, v165\n" \
  "v_max3_u32 v164, v112, v113, v114\n" \
  "v_max_u32 v164, v164, v115\n" \
  "v_cmp_eq_u32 s[60:61], -1, v164\n" \
  "v_max3_u32 v165, v116, v117, v118\n" \
  "v_max_u32 v165, v165, v119\n" \
  "v_cmp_eq_u32 s[62:63], -1, v165\n" \
  "v_max3_u32 v164, v120, v121, v122\n" \
  "v_max_u32 v164, v164, v123\n" \
  "v_cmp_eq_u32 s[64:65], -1, v164\n" \
  "v_max3_u32 v165, v124, v125, v126\n" \
  "v_max_u32 v165, v165, v127\n" \
  "v_cmp_eq_u32 s[66:67], -1, v165\n" \
  "s_mov_b32 %[todo], 0\n" \
  "s_cmp_lg_u64 s[66:67], 0\n" \
  "s_addc_u32 %[todo], %[todo], %[todo]\n" \
  "s_cmp_lg_u64 s[64:65], 0\n" \
  "s_addc_u32 %[todo], %[todo], %[todo]\n" \
  "s_cmp_lg_u64 s[62:63], 0\n" \
  "s_addc_u32 %[todo], %[todo], %[todo]\n" \
  "s_cmp_lg_u64 s[60:61], 0\n" \
  "s_addc_u32 %[todo], %[todo], %[todo]\n" \
  "s_cmp_lg_u64 s[58:59], 0\n" \
  "s_addc_u32 %[todo], %[todo], %[todo]\n" \
  "s_cmp_lg_u64 s[56:57], 0\n" \
  "s_addc_u32 %[todo], %[todo], %[todo]\n" \
  "s_cmp_lg_u64 s[54:55], 0\n" \
  "s_addc_u32 %[todo], %[todo], %[todo]\n" \
  "s_cmp_lg_u64 s[52:53], 0\n" \
  "s_addc_u32 %[todo], %[todo], %[todo]\n" \
  "s_cmp_lg_u64 s[50:51], 0\n" \
  "s_addc_u32 %[todo], %[todo], %[todo]\n" \
  "s_cmp_lg_u64 s[48:49], 0\n" \
  "s_addc_u32 %[todo], %[todo], %[todo]\n" \
  "s_cmp_lg_u64 s[46:47], 0\n" \
  "s_addc_u32 %[todo], %[todo], %[todo]\n" \
  "s_cmp_lg_u64 s[44:45], 0\n" \
  "s_addc_u32 %[todo], %[todo], %[todo]\n" \
  "s_cmp_lg_u64 s[42:43], 0\n" \
  "s_addc_u32 %[todo], %[todo], %[todo]\n" \
  "s_cmp_lg_u64 s[40:41], 0\n" \
  "s_addc_u32 %[todo], %[todo], %[todo]\n" \
  "s_cmp_lg_u64 s[38:39], 0\n" \
  "s_addc_u32 %[todo], %[todo], %[todo]\n" \
  "s_cmp_lg_u64 s[36:37], 0\n" \
  "s_addc_u32 %[todo], %[todo], %[todo]\n" \
  "s_or_b32 %[t], %[todo], 0x10000\n" \
  "s_ff1_i32_b32 %[t], %[t]\n" \
  "s_cmp_eq_u32 %[t], 0\n" \
  "s_cbranch_scc1 Lt_stop%=\n" \
  "s_cmp_gt_u32 %[nx], 0\n" \
  "s_cbranch_scc1 Lt_m0_%=\n" \
  "s_waitcnt lgkmcnt(12)\n" \
  "v_mfma_f32_32x32x16_bf16 v[32:47], v[128:131], v[64:67], v[32:47]\n" \
  "v_mfma_f32_32x32x16_bf16 v[48:63], v[132:135], v[64:67], v[48:63]\n" \
  "ds_read_b64_tr_b16 v[128:129], %[la] offset:8192\n" \
  "ds_read_b64_tr_b16 v[130:131], %[la] offset:9216\n" \
  "ds_read_b64_tr_b16 v[132:133], %[lb] offset:8192\n" \
  "ds_read_b64_tr_b16 v[134:135], %[lb] offset:9216\n" \
  "Lt_m0_%=:\n" \
  "s_cmp_eq_u32 %[t], 1\n" \
  "s_cbranch_scc1 Lt_stop%=\n" \
  "s_cmp_gt_u32 %[nx], 1\n" \
  "s_cbranch_scc1 Lt_m1_%=\n" \
  "s_waitcnt lgkmcnt(12)\n" \
  "v_mfma_f32_32x32x16_bf16 v[32:47], v[136:139], v[68:71], v[32:47]\n" \
  "v_mfma_f32_32x32x16_bf16 v[48:63], v[140:143], v[68:71], v[48:63]\n" \
  "ds_read_b64_tr_b16 v[136:137], %[la] offset:10240\n" \
  "ds_read_b64_tr_b16 v[138:139], %[la] offset:11264\n" \
  "ds_read_b64_tr_b16 v[140:141], %[lb] offset:10240\n" \
  "ds_read_b64_tr_b16 v[142:143], %[lb] offset:11264\n" \
  "Lt_m1_%=:\n" \
  "s_cmp_eq_u32 %[t], 2\n" \
  "s_cbranch_scc1 Lt_stop%=\n" \
  "s_cmp_gt_u32 %[nx], 2\n" \
  "s_cbranch_scc1 Lt_m2_%=\n" \
  "s_waitcnt lgkmcnt(12)\n" \
  "v_mfma_f32_32x32x16_bf16 v[32:47], v[144:147], v[72:75], v[32:47]\n" \
  "v_mfma_f32_32x32x16_bf16 v[48:63], v[148:151], v[72:75], v[48:63]\n" \
  "ds_read_b64_tr_b16 v[144:145], %[la] offset:12288\n" \
  "ds_read_b64_tr_b16 v[146:147], %[la] offset:13312\n" \
  "ds_read_b64_tr_b16 v[148:149], %[lb] offset:12288\n" \
  "ds_read_b64_tr_b16 v[150:151], %[lb] offset:13312\n" \
  "Lt_m2_%=:\n" \
  "s_cmp_eq_u32 %[t], 3\n" \
  "s_cbranch_scc1 Lt_stop%=\n" \
  "s_cmp_gt_u32 %[nx], 3\n" \
  "s_cbranch_scc1 Lt_m3_%=\n" \
  "s_waitcnt lgkmcnt(12)\n" \
  "v_mfma_f32_32x32x16_bf16 v[32:47], v[152:155], v[76:79], v[32:47]\n" \
  "v_mfma_f32_32x32x16_bf16 v[48:63], v[156:159], v[76:79], v[48:63]\n" \
  "ds_read_b64_tr_b16 v[152:153], %[la] offset:14336\n" \
  "ds_read_b64_tr_b16 v[154:155], %[la] offset:15360\n" \
  "ds_read_b64_tr_b16 v[156:157], %[lb] offset:14336\n" \
  "ds_read_b64_tr_b16 v[158:159], %[lb] offset:15360\n" \
  "Lt_m3_%=:\n" \
  "s_cmp_eq_u32 %[t], 4\n" \
  "s_cbranch_scc1 Lt_stop%=\n" \
  "s_cmp_gt_u32 %[nx], 4\n" \
  "s_cbranch_scc1 Lt_m4_%=\n" \
  "s_waitcnt lgkmcnt(12)\n" \
  "v_mfma_f32_32x32x16_bf16 v[32:47], v[128:131], v[80:83], v[32:47]\n" \
  "v_mfma_f32_32x32x16_bf16 v[48:63], v[132:135], v[80:83], v[48:63]\n" \
  "ds_read_b64_tr_b16 v[128:129], %[la] offset:16384\n" \
  "ds_read_b64_tr_b16 v[130:131], %[la] offset:17408\n" \
  "ds_read_b64_tr_b16 v[132:133], %[lb] offset:16384\n" \
  "ds_read_b64_tr_b16 v[134:135], %[lb] offset:17408\n" \
  "Lt_m4_%=:\n" \
  "s_cmp_eq_u32 %[t], 5\n" \
  "s_cbranch_scc1 Lt_stop%=\n" \
  "s_cmp_gt_u32 %[nx], 5\n" \
  "s_cbranch_scc1 Lt_m5_%=\n" \
  "s_waitcnt lgkmcnt(12)\n" \
  "v_mfma_f32_32x32x16_bf16 v[32:47], v[136:139], v[84:87], v[32:47]\n" \
  "v_mfma_f32_32x32x16_bf16 v[48:63], v[140:143], v[84:87], v[48:63]\n" \
  "ds_read_b64_tr_b16 v[136:137], %[la] offset:18432\n" \
  "ds_read_b64_tr_b16 v[138:139], %[la] offset:19456\n" \
  "ds_read_b64_tr_b16 v[140:141], %[lb] offset:18432\n" \
  "ds_read_b64_tr_b16 v[142:143], %[lb] offset:19456\n" \
  "Lt_m5_%=:\n" \
  "s_cmp_eq_u32 %[t], 6\n" \
  "s_cbranch_scc1 Lt_stop%=\n" \
  "s_cmp_gt_u32 %[nx], 6\n" \
  "s_cbranch_scc1 Lt_m6_%=\n" \
  "s_waitcnt lgkmcnt(12)\n" \
  "v_mfma_f32_32x32x16_bf16 v[32:47], v[144:147], v[88:91], v[32:47]\n" \
  "v_mfma_f32_32x32x16_bf16 v[48:63], v[148:151], v[88:91], v[48:63]\n" \
  "ds_read_b64_tr_b16 v[144:145], %[la] offset:20480\n" \
  "ds_read_b64_tr_b16 v[146:147], %[la] offset:21504\n" \
  "ds_read_b64_tr_b16 v[148:149], %[lb] offset:20480\n" \
  "ds_read_b64_tr_b16 v[150:151], %[lb] offset:21504\n" \
  "Lt_m6_%=:\n" \
  "s_cmp_eq_u32 %[t], 7\n" \
  "s_cbranch_scc1 Lt_stop%=\n" \
  "s_cmp_gt_u32 %[nx], 7\n" \
  "s_cbranch_scc1 Lt_m7_%=\n" \
  "s_waitcnt lgkmcnt(12)\n" \
  "v_mfma_f32_32x32x16_bf16 v[32:47], v[152:155], v[92:95], v[32:47]\n" \
  "v_mfma_f32_32x32x16_bf16 v[48:63], v[156:159], v[92:95], v[48:63]\n" \
  "ds_read_b64_tr_b16 v[152:153], %[la] offset:22528\n" \
  "ds_read_b64_tr_b16 v[154:155], %[la] offset:23552\n" \
  "ds_read_b64_tr_b16 v[156:157], %[lb] offset:22528\n" \
  "ds_read_b64_tr_b16 v[158:159], %[lb] offset:23552\n" \
  "Lt_m7_%=:\n" \
  "s_cmp_eq_u32 %[t], 8\n" \
  "s_cbranch_scc1 Lt_stop%=\n" \
  "s_cmp_gt_u32 %[nx], 8\n" \
  "s_cbranch_scc1 Lt_m8_%=\n" \
  "s_waitcnt lgkmcnt(12)\n" \
  "v_mfma_f32_32x32x16_bf16 v[32:47], v[128:131], v[96:99], v[32:47]\n" \
  "v_mfma_f32_32x32x16_bf16 v[48:63], v[132:135], v[96:99], v[48:63]\n" \
  "ds_read_b64_tr_b16 v[128:129], %[la] offset:24576\n" \
  "ds_read_b64_tr_b16 v[130:131], %[la] offset:25600\n" \
  "ds_read_b64_tr_b16 v[132:133], %[lb] offset:24576\n" \
  "ds_read_b64_tr_b16 v[134:135], %[lb] offset:25600\n" \
  "Lt_m8_%=:\n" \
  "s_cmp_eq_u32 %[t], 9\n" \
  "s_cbranch_scc1 Lt_stop%=\n" \
  "s_cmp_gt_u32 %[nx], 9\n" \
  "s_cbranch_scc1 Lt_m9_%=\n" \
  "s_waitcnt lgkmcnt(12)\n" \
  "v_mfma_f32_32x32x16_bf16 v[32:47], v[136:139], v[100:103], v[32:47]\n" \
  "v_mfma_f32_32x32x16_bf16 v[48:63], v[140:143], v[100:103], v[48:63]\n" \
  "ds_read_b64_tr_b16 v[136:137], %[la] offset:26624\n" \
  "ds_read_b64_tr_b16 v[138:139], %[la] offset:27648\n" \
  "ds_read_b64_tr_b16 v[140:141], %[lb] offset:26624\n" \
  "ds_read_b64_tr_b16 v[142:143], %[lb] offset:27648\n" \
  "Lt_m9_%=:\n" \
  "s_cmp_eq_u32 %[t], 10\n" \
  "s_cbranch_scc1 Lt_stop%=\n" \
  "s_cmp_gt_u32 %[nx], 10\n" \
  "s_cbranch_scc1 Lt_m10_%=\n" \
  "s_waitcnt lgkmcnt(12)\n" \
  "v_mfma_f32_32x32x16_bf16 v[32:47], v[144:147], v[104:107], v[32:47]\n" \
  "v_mfma_f32_32x32x16_bf16 v[48:63], v[148:151], v[104:107], v[48:63]\n" \
  "ds_read_b64_tr_b16 v[144:145], %[la] offset:28672\n" \
  "ds_read_b64_tr_b16 v[146:147], %[la] offset:29696\n" \
  "ds_read_b64_tr_b16 v[148:149], %[lb] offset:28672\n" \
  "ds_read_b64_tr_b16 v[150:151], %[lb] offset:29696\n" \
  "Lt_m10_%=:\n" \
  "s_cmp_eq_u32 %[t], 11\n" \
  "s_cbranch_scc1 Lt_stop%=\n" \
  "s_cmp_gt_u32 %[nx], 11\n" \
  "s_cbranch_scc1 Lt_m11_%=\n" \
  "s_waitcnt lgkmcnt(12)\n" \
  "v_mfma_f32_32x32x16_bf16 v[32:47], v[152:155], v[108:111], v[32:47]\n" \
  "v_mfma_f32_32x32x16_bf16 v[48:63], v[156:159], v[108:111], v[48:63]\n" \
  "ds_read_b64_tr_b16 v[152:153], %[la] offset:30720\n" \
  "ds_read_b64_tr_b16 v[154:155], %[la] offset:31744\n" \
  "ds_read_b64_tr_b16 v[156:157], %[lb] offset:30720\n" \
  "ds_read_b64_tr_b16 v[158:159], %[lb] offset:31744\n" \
  "Lt_m11_%=:\n" \
  "s_cmp_eq_u32 %[t], 12\n" \
  "s_cbranch_scc1 Lt_stop%=\n" \
  "s_cmp_gt_u32 %[nx], 12\n" \
  "s_cbranch_scc1 Lt_m12_%=\n" \
  "s_waitcnt lgkmcnt(12)\n" \
  "v_mfma_f32_32x32x16_bf16 v[32:47], v[128:131], v[112:115], v[32:47]\n" \
  "v_mfma_f32_32x32x16_bf16 v[48:63], v[132:135], v[112:115], v[48:63]\n" \
  "Lt_m12_%=:\n" \
  "s_cmp_eq_u32 %[t], 13\n" \
  "s_cbranch_scc1 Lt_stop%=\n" \
  "s_cmp_gt_u32 %[nx], 13\n" \
  "s_cbranch_scc1 Lt_m13_%=\n" \
  "s_waitcnt lgkmcnt(8)\n" \
  "v_mfma_f32_32x32x16_bf16 v[32:47], v[136:139], v[116:119], v[32:47]\n" \
  "v_mfma_f32_32x32x16_bf16 v[48:63], v[140:143], v[116:119], v[48:63]\n" \
  "Lt_m13_%=:\n" \
  "s_cmp_eq_u32 %[t], 14\n" \
  "s_cbranch_scc1 Lt_stop%=\n" \
  "s_cmp_gt_u32 %[nx], 14\n" \
  "s_cbranch_scc1 Lt_m14_%=\n" \
  "s_waitcnt lgkmcnt(4)\n" \
  "v_mfma_f32_32x32x16_bf16 v[32:47], v[144:147], v[120:123], v[32:47]\n" \
  "v_mfma_f32_32x32x16_bf16 v[48:63], v[148:151], v[120:123], v[48:63]\n" \
  "Lt_m14_%=:\n" \
  "s_cmp_eq_u32 %[t], 15\n" \
  "s_cbranch_scc1 Lt_stop%=\n" \
  "s_cmp_gt_u32 %[nx], 15\n" \
  "s_cbranch_scc1 Lt_m15_%=\n" \
  "s_waitcnt lgkmcnt(0)\n" \
  "v_mfma_f32_32x32x16_bf16 v[32:47], v[152:155], v[124:127], v[32:47]\n" \
  "v_mfma_f32_32x32x16_bf16 v[48:63], v[156:159], v[124:127], v[48:63]\n" \
  "Lt_m15_%=:\n" \
  "s_branch Lt_done%=\n" \
  "Lt_stop%=:\n" \
  "s_mov_b32 %[nx], %[t]\n" \
  "Lt_next%=:\n" \
  "s_sub_u32 %[sp], %[sp], 1\n" \
  "s_cmp_lg_u32 %[sp], 0\n" \
  "s_cbranch_scc1 Lt_loop%=\n" \
  "Lt_done%=:\n" \
  "s_waitcnt lgkmcnt(0)\n" \
  "s_nop 11\n"
#define TAIL_CLOB "v64", "v65", "v66", "v67", "v68", "v69", "v70", "v71", "v72", "v73", "v74", "v75", "v76", "v77", "v78", "v79", "v80", "v81", "v82", "v83", "v84", "v85", "v86", "v87", "v88", "v89", "v90", "v91", "v92", "v93", "v94", "v95", "v96", "v97", "v98", "v99", "v100", "v101", "v102", "v103", "v104", "v105", "v106", "v107", "v108", "v109", "v110", "v111", "v112", "v113", "v114", "v115", "v116", "v117", "v118", "v119", "v120", "v121", "v122", "v123", "v124", "v125", "v126", "v127", "v128", "v129", "v130", "v131", "v132", "v133", "v134", "v135", "v136", "v137", "v138", "v139", "v140", "v141", "v142", "v143", "v144", "v145", "v146", "v147", "v148", "v149", "v150", "v151", "v152", "v153", "v154", "v155", "v156", "v157", "v158", "v159", "v160", "v161", "v162", "v163", "v164", "v165", "s36", "s37", "s38", "s39", "s40", "s41", "s42", "s43", "s44", "s45", "s46", "s47", "s48", "s49", "s50", "s51", "s52", "s53", "s54", "s55", "s56", "s57", "s58", "s59", "s60", "s61", "s62", "s63", "s64", "s65", "s66", "s67"

__global__ __launch_bounds__(768, 3) void fused_k(const char* __restrict__ x, const char* __restrict__ wqkv,
                                                  const char* __restrict__ wo, const char* __restrict__ w1,
                                                  const char* __restrict__ b1, const char* __restrict__ w2,
                                                  const char* __restrict__ b2, const char* __restrict__ gw,
                                                  char* ws, char* __restrict__ out) {
  extern __shared__ __attribute__((aligned(16))) char smem[];
  const int tid = threadIdx.x;
  const int wid = __builtin_amdgcn_readfirstlane(tid >> 6);
  const int xg = blockIdx.x & 7, ig = blockIdx.x >> 3;
  const int qb = (xg & 1) * 4 + (ig & 3), h = (xg >> 1) * 8 + (ig >> 2);
  const int hf = h < 28 ? h : 27;
  const unsigned lds0 = __builtin_amdgcn_readfirstlane((unsigned)(uintptr_t)smem);
  lds_ptr L = (lds_ptr)smem;
  const __amdgpu_buffer_rsrc_t rs = __builtin_amdgcn_make_buffer_rsrc(ws, 0, WS_END, 0x00020000);
  const int u = h >> 3, ct = h & 7;
  const lds_cptr cnt = (lds_cptr)L + L_CNT;
  STAMP_DECL
  STAMP(0);
  if (tid < 32) *(LAS unsigned*)(L + L_CNT + tid * 4) = tid == C_READY + 8 ? 8u : 0u;

  const int j = wid & 3;
  const char* xs = x + (size_t)(qb * 256 + j * 64) * 1024;
  const char* wq = wqkv + h * 32 + (size_t)j * 16 * 3072;
  const char* ww = w1 + hf * 128 + (size_t)j * 16 * (FF * 2);
#define DMA_X(c, sb, i0) { _Pragma("unroll") for (int i = (i0); i < (i0) + 4; ++i)                              \
      glds16(xs + (c) * 128 + i * 8192, (i & 1) ? xv1 : xv0, (sb) + (j * 8 + i) * 1024); }
#define DMA_QKV(c, sb) { glds16(wq + (size_t)(c) * 64 * 3072, qv_, (sb) + ST_QK + j * 1024);                    \
      if (lane < 32) glds16(wq + (size_t)(c) * 64 * 3072, vv_, (sb) + ST_V + j * 512); }
#define DMA_W1(c, sb) { glds16(ww + (size_t)(c) * 64 * (FF * 2), wv0, (sb) + ST_W1 + j * 2048);                 \
      glds16(ww + (size_t)((c) * 64 + 8) * (FF * 2), wv1, (sb) + ST_W1 + j * 2048 + 1024); }

  if (wid < 8) {
    LANE_VARS DMA_VARS
    bf16x8 qf;
    int tile;
    if (wid >= 4) {
      __builtin_amdgcn_s_setprio(3);
      __builtin_amdgcn_s_sleep(LDELAY);
      unsigned so = 2 * ST_BYTES;
#pragma unroll 1
      for (int c = 1; c < 8; ++c) {
        const unsigned sb = lds0 + so;
        DMA_QKV(c, sb) DMA_X(c, sb, 0)
        asm volatile("s_waitcnt vmcnt(6)\n\ts_barrier" ::: "memory");
        DMA_X(c, sb, 4) DMA_W1(c, sb)
        so = so == 2 * ST_BYTES ? 0 : so + ST_BYTES;
      }
      asm volatile("s_waitcnt vmcnt(0)\n\ts_barrier" ::: "memory");
      STAMP(1);
      tile = qb * 8 + 2 * j + 1;
      lds_wait(cnt + C_READY * 4, 8);
      STAMP(8);
      qf = ld128((lds_cptr)L + L_QX + j * 1024 + lane * 16);
    } else {
      DMA_QKV(0, lds0 + ST_BYTES) DMA_X(0, lds0 + ST_BYTES, 0)
      __builtin_amdgcn_s_setprio(QPRIO);
      if (wid == 0 && h >= 28) *(LAS u32x4*)(L + L_GATE + lane * 16) = *(const u32x4*)(gw + lane * 16);
      if (wid == 1 && lane == 0) *(LAS u64*)(L + L_E) = __hip_atomic_load((gu64*)(ws + WS_E), RLX_AGENT);
      const unsigned xo0 = (wid * 64 + r16) * 128 + ((g4 ^ (r16 >> 1)) << 4), xo1 = (wid * 64 + r16) * 128 + (((4 + g4) ^ (r16 >> 1)) << 4);
      const unsigned qo = ST_QK + (8 * g4 + q4) * 64 + g1 * 32 + p4 * 8, ko = qo ^ 32;
      const unsigned vo0 = ST_V + (8 * g4 + 4 * g1 + q4) * 32 + p4 * 8, vo1 = vo0 ^ 128;
      f32x4 aq[4], ak[4], av[4];
#pragma unroll
      for (int m = 0; m < 4; ++m) { aq[m] = f32x4{}; ak[m] = f32x4{}; av[m] = f32x4{}; }
      bf16x8 fx[2][4], fq[2], fk[2], fv[2];
#define LOAD_A(b, SB) { const lds_cptr B_ = (lds_cptr)L + (SB);                                                \
        fq[b] = ldtr2(B_ + qo + (b) * 2048, B_ + qo + (b) * 2048 + 256);                                       \
        _Pragma("unroll") for (int m = 0; m < 4; ++m) fx[b][m] = ld128(B_ + ((b) ? xo1 : xo0) + m * 2048);     \
        fk[b] = ldtr2(B_ + ko + (b) * 2048, B_ + ko + (b) * 2048 + 256);                                       \
        fv[b] = ldtr2(B_ + vo0 + (b) * 1024, B_ + vo1 + (b) * 1024); }
#define MMA_A(b) { _Pragma("unroll") for (int m = 0; m < 4; ++m) aq[m] = MFMA16(fq[b], fx[b][m], aq[m]);       \
        _Pragma("unroll") for (int m = 0; m < 4; ++m) ak[m] = MFMA16(fk[b], fx[b][m], ak[m]);                  \
        _Pragma("unroll") for (int m = 0; m < 4; ++m) av[m] = MFMA16(fx[b][m], fv[b], av[m]); }
      asm volatile("s_waitcnt vmcnt(0) lgkmcnt(0)\n\ts_barrier" ::: "memory");
      STAMP(10);
      int sb = ST_BYTES;
      LOAD_A(0, ST_BYTES)
      tile = qb * 8 + 2 * wid;
      const unsigned kvo = WS_KV + (h * 64 + tile) * 2048 + lane * 16;
#pragma unroll 1
      for (int c = 0; c < 8; ++c) {
        SBAR();
        LOAD_A(1, sb) MMA_A(0) INTERLEAVE(10)
        SBAR();
        sb = sb == 2 * ST_BYTES ? 0 : sb + ST_BYTES;
        if (c < 7) BAR();
        SBAR();
        LOAD_A(0, sb) MMA_A(1) INTERLEAVE(10)
        SBAR();
      }
      lds_arrive(lds0 + L_CNT + C_DONE * 4, 1, lane);
      STAMP(1);
      __builtin_amdgcn_s_setprio(3);
      const float qs = 0.25f * 1.4426950408889634f;
#pragma unroll
      for (int mt = 0; mt < 2; ++mt) {
        const u32x4 kv = merge16(ak[2 * mt], ak[2 * mt + 1], 1.0f), vv = merge32(av[2 * mt], av[2 * mt + 1]);
        st_sc1(kv, rs, kvo + mt * 2048); st_sc1(vv, rs, kvo + mt * 2048 + 1024);
        *(LAS u32x4*)(L + (2 * wid + mt) * 2048 + lane * 16) = kv;
        *(LAS u32x4*)(L + (2 * wid + mt) * 2048 + 1024 + lane * 16) = vv;
      }
      STAMP(5);
      *(LAS u32x4*)(L + L_QX + wid * 1024 + lane * 16) = merge16(aq[2], aq[3], qs);
      lds_arrive(lds0 + L_CNT + C_READY * 4, 2, lane);
      STAMP(6);
      qf = __builtin_bit_cast(bf16x8, merge16(aq[0], aq[1], qs));
      flag_set(ws, WS_FKV + ((h * 4 + wid) * 8 + qb) * 8, make_tag(*(const LAS u64*)(L + L_E)), lane);
      STAMP(7);
      lds_wait(cnt + C_READY * 4, 8);
      STAMP(8);
    }
    const lds_cptr kp = (lds_cptr)L + lane * 16;
    f32x16 negm, o = f32x16{}, sa;
    {
      const bf16x8 k0 = ld128(kp);
      const f32x16 s0 = MFMA(k0, qf, f32x16{});
      const float m = swap_max(max16(s0));
#pragma unroll
      for (int r = 0; r < 16; ++r) negm[r] = -m;
      sa = MFMA(k0, qf, negm);
    }
    const unsigned one = (r32 == 16 || r32 == 20) ? 0x3f803f80u : 0u;
    bf16x8 kf = ld128(kp + 2048), v0 = __builtin_bit_cast(bf16x8, u32x4{one, one, one, one}), v1 = v0, p0 = bf16x8{}, p1 = bf16x8{};
    unsigned ka = lds0 + lane * 16, va_ = lds0 + 1024 + hh * 256 + r16 * 16, ca = lds0 + L_CNT + C_READY * 4;
    STAMP(2);
    float g = 0.f;
#pragma unroll 1
    for (int seg = 0; seg < 4; ++seg) {
      unsigned n_ = seg == 1 ? 4 : (seg == 2 ? 2 : 1), t_, sp_;
      if (seg < 2) __builtin_amdgcn_s_setprio(2); else if (seg == 2) __builtin_amdgcn_s_setprio(1); else __builtin_amdgcn_s_setprio(0);
      asm volatile(ATT_ASM
        : "+{v[64:79]}"(sa), "+{v[112:127]}"(o), "+{v[132:135]}"(kf), "+{v[136:139]}"(v0), "+{v[140:143]}"(v1), "+{v[144:147]}"(p0),
          "+{v[148:151]}"(p1), "+{v152}"(ka), "+{v153}"(va_), "+{v154}"(ca), [n] "+s"(n_), [tmp] "=&s"(t_), [spin] "=&s"(sp_)
        : "{v[96:111]}"(negm), "{v[128:131]}"(qf), [msk] "s"(0x0000ffff0000ffffull)
        : "v80", "v81", "v82", "v83", "v84", "v85", "v86", "v87", "v88", "v89", "v90", "v91", "v92", "v93", "v94", "v95",
          "v155", "scc", "memory");
      if (seg == 0) {
        lds_wait(cnt + (C_READY + 1) * 4, 8);
        sa = MFMA(ld128(kp + 16384), qf, negm);
        kf = ld128(kp + 16384 + 2048);
        ca = lds0 + L_CNT + (C_READY + 2) * 4;
      } else if (seg == 1) {
        lds_arrive(lds0 + L_CNT + C_HALF * 4, 1, lane);
      } else if (seg == 2) {
        lds_arrive(lds0 + L_CNT + C_LATE * 4, 1, lane);
        g = gate_from_logit(*(const LAS float*)(L + L_G + ((tile & 7) * 32 + r32) * 4));
      }
    }
    __builtin_amdgcn_s_setprio(3);
    o = MFMA(v0, p0, o);
    o = MFMA(v1, p1, o);
    bool ok = o[8] < 1.2676506e30f;
#pragma unroll
    for (int r = 0; r < 8; ++r) ok = ok && (fabsf(o[r]) < 3.0e38f);
    if (__builtin_expect(!__all(ok), 0)) {
      flags_wait(ws, WS_FKV + (h * 32 + r32) * 8, make_tag(*(const LAS u64*)(L + L_E)));
      float m = -INFINITY;
      o = f32x16{};
#pragma unroll 1
      for (int t = 0; t < 64; ++t) {
        const unsigned tb = WS_KV + (h * 64 + t) * 2048;
        const u32x4 kk = ld_sc1(rs, tb + lane * 16, 0);
        u32x4 va = ld_sc1(rs, tb + 1024 + hh * 256 + r16 * 16, 0), vb = ld_sc1(rs, tb + 1536 + hh * 256 + r16 * 16, 0);
        if (g1) { va = u32x4{one, one, one, one}; vb = va; }
        f32x16 z = MFMA(__builtin_bit_cast(bf16x8, kk), qf, f32x16{});
        const float mn = fmaxf(m, swap_max(max16(z)));
        const float al = __builtin_amdgcn_exp2f(m - mn);
        m = mn;
#pragma unroll
        for (int r = 0; r < 16; ++r) { z[r] = __builtin_amdgcn_exp2f(z[r] - m); o[r] *= al; }
        bf16x8 pa, pb; pack_p(z, pa, pb);
        o = MFMA(__builtin_bit_cast(bf16x8, va), pa, o);
        o = MFMA(__builtin_bit_cast(bf16x8, vb), pb, o);
      }
    }
    const float sc = g * __builtin_amdgcn_rcpf(o[8]);
    u32x4 ov;
#pragma unroll
    for (int i = 0; i < 4; ++i) ov[i] = cvtpk(o[2 * i] * sc, o[2 * i + 1] * sc);
    st_sc1(not_empty(ov), rs, WS_A + (tile * KSTEPS + h) * 1024 + lane * 16);
    STAMP(3);
    STAMP_OUT;
  } else {
    const int v = wid - 8;
    LANE_VARS DMA_VARS
    DMA_W1(0, lds0 + ST_BYTES) DMA_X(0, lds0 + ST_BYTES, 4)
    f32x4 ah[4][4];
    {
#pragma unroll
      for (int i = 0; i < 4; ++i) {
        const u32x2 bw = *(const u32x2*)(b1 + (hf * 64 + i * 16 + 4 * g4) * 2);
        ah[i][0] = f32x4{__uint_as_float(bw[0] << 16), __uint_as_float(bw[0] & 0xffff0000u), __uint_as_float(bw[1] << 16), __uint_as_float(bw[1] & 0xffff0000u)};
        ah[i][1] = ah[i][0]; ah[i][2] = ah[i][0]; ah[i][3] = ah[i][0];
      }
      const unsigned xo0 = (v * 64 + r16) * 128 + ((g4 ^ (r16 >> 1)) << 4), xo1 = (v * 64 + r16) * 128 + (((4 + g4) ^ (r16 >> 1)) << 4);
      const unsigned wo0 = ST_W1 + (8 * g4 + q4) * 128 + (((q4 >> 1) | (2 * g1)) * 32) + p4 * 8;
      bf16x8 fx[2][4], fw[2][4];
#define LOAD_F(b, SB) { const lds_cptr B_ = (lds_cptr)L + (SB);                                                \
        _Pragma("unroll") for (int m = 0; m < 4; ++m) {                                                        \
          fw[b][m] = ldtr2(B_ + (wo0 ^ (m * 32)) + (b) * 4096, B_ + (wo0 ^ (m * 32)) + (b) * 4096 + 512);      \
          fx[b][m] = ld128(B_ + ((b) ? xo1 : xo0) + m * 2048); } }
#define MMA_F(b) { _Pragma("unroll") for (int i = 0; i < 4; ++i) _Pragma("unroll") for (int m = 0; m < 4; ++m) \
          ah[i][m] = MFMA16(fw[b][i], fx[b][m], ah[i][m]); }
      asm volatile("s_waitcnt vmcnt(0) lgkmcnt(0)\n\ts_barrier" ::: "memory");
      if (h < 28) {
        int sb = ST_BYTES;
      LOAD_F(0, ST_BYTES)
#pragma unroll 1
      for (int c = 0; c < 8; ++c) {
        SBAR();
        LOAD_F(1, sb) MMA_F(0) INTERLEAVE(12)
        SBAR();
        sb = sb == 2 * ST_BYTES ? 0 : sb + ST_BYTES;
        if (c < 7) BAR();
        SBAR();
        LOAD_F(0, sb) MMA_F(1) INTERLEAVE(12)
        SBAR();
      }
      lds_arrive(lds0 + L_CNT + C_DONE * 4, 1, lane);
        STAMP(1);
      } else {
        unsigned go = r16 == 0 ? L_GATE + 16 * g4 : L_ZERO;
        const unsigned gstep = r16 == 0 ? 64 : 0;
        int sb = ST_BYTES;
#pragma unroll
        for (int m = 0; m < 4; ++m) ah[0][m] = f32x4{};
#pragma unroll 1
        for (int c = 0; c < 8; ++c) {
#pragma unroll
          for (int b = 0; b < 2; ++b) {
            const bf16x8 fg = ld128((lds_cptr)L + go); go += gstep;
#pragma unroll
            for (int m = 0; m < 4; ++m) ah[0][m] = MFMA16(fg, ld128((lds_cptr)L + sb + (b ? xo1 : xo0) + m * 2048), ah[0][m]);
          }
          sb = sb == 2 * ST_BYTES ? 0 : sb + ST_BYTES;
          if (c < 7) BAR();
        }
        lds_arrive(lds0 + L_CNT + C_DONE * 4, 1, lane);
        if (h == 28 && lane < 16) {
#pragma unroll
          for (int m = 0; m < 4; ++m)
            __hip_atomic_store((GAS float*)(ws + WS_G) + qb * 256 + v * 64 + m * 16 + lane, ah[0][m][0], RLX_AGENT);
        }
      }
    }
    __builtin_amdgcn_s_setprio(3);
    const u64 tag = make_tag(*(const LAS u64*)(L + L_E));
    STAMP(2);
    {
      const lds_ptr kw = L + v * 4096 + lane * 16;
      const unsigned kvb = WS_KV + (h * 64 + 2 * v) * 2048, fkv = WS_FKV + (h * 4 + v) * 64;
      u32x4 hp[8];
#pragma unroll
      for (int mt = 0; mt < 2; ++mt)
#pragma unroll
        for (int i = 0; i < 4; ++i) {
          f32x4 a = ah[i][2 * mt], b = ah[i][2 * mt + 1];
#pragma unroll
          for (int r = 0; r < 4; ++r) { a[r] = fmaxf(a[r], 0.f); a[r] *= a[r]; b[r] = fmaxf(b[r], 0.f); b[r] *= b[r]; }
          hp[mt * 4 + i] = merge16(a, b, 1.0f); PIN(hp[mt * 4 + i]);
        }
      gu64* fl = (gu64*)(ws + fkv + ((qb + lane) & 7) * 8);
      unsigned got = 0;
#pragma unroll 1
      for (unsigned spins = 0; spins < (SPIN_CAP >> 4); ++spins) {
        got = (unsigned)__ballot(__hip_atomic_load(fl, RLX_AGENT) == tag) & 0xfeu;
        if (got & 2) break;
        __builtin_amdgcn_s_sleep(1);
      }
      asm volatile("" ::: "memory");
      {
        u32x4 kr[GA][4];
#pragma unroll
        for (int c = 0; c < GA; ++c) {
          const unsigned vo = lane * 16 + ((got >> (c + 1) & 1) ? kvb + ((qb + 1 + c) & 7) * 16384 : OOB);
#pragma unroll
          for (int i = 0; i < 4; ++i) kr[c][i] = ld_sc1(rs, vo + i * 1024, 0);
        }
        const u64 f2 = __hip_atomic_load(fl, RLX_AGENT);
        SBAR();
#pragma unroll
        for (int i = 0; i < 8; ++i)
          st_sc1(hp[i], rs, (h < 28 ? WS_A + ((qb * 8 + v * 2 + (i >> 2)) * KSTEPS + 32 + h * 4 + (i & 3)) * 1024 : OOB) + lane * 16);
#pragma unroll
        for (int i = 0; i < 2; ++i) st_sc1(u32x4{~0u, ~0u, ~0u, ~0u}, rs, WS_A + ((qb * 8 + v * 2 + i) * KSTEPS + h) * 1024 + lane * 16);
        SBAR();
        lds_wait(cnt + C_DONE * 4, 8);
#pragma unroll
        for (int c = 0; c < GA; ++c)
          if (got >> (c + 1) & 1) {
#pragma unroll
            for (int i = 0; i < 4; ++i) *(LAS u32x4*)(kw + (1 + c) * 16384 + i * 1024) = kr[c][i];
            lds_arrive(lds0 + L_CNT + (C_READY + 1 + c) * 4, 2, lane);
          }
        if (!__all(f2 == tag)) flags_wait(ws, fkv + (lane & 7) * 8, tag);
        asm volatile("" ::: "memory");
      }
      STAMP(3);
      {
        u32x4 kr[6][4];
#pragma unroll
        for (int c = 0; c < 6; ++c) {
          const unsigned vo = lane * 16 + ((c + 1 < GA && (got >> (c + 2) & 1)) ? OOB : kvb + ((qb + 2 + c) & 7) * 16384);
#pragma unroll
          for (int i = 0; i < 4; ++i) kr[c][i] = ld_sc1(rs, vo + i * 1024, 0);
        }
#pragma unroll
        for (int c = 0; c < 6; ++c)
          if (!(c + 1 < GA && (got >> (c + 2) & 1))) {
#pragma unroll
            for (int i = 0; i < 4; ++i) *(LAS u32x4*)(kw + (2 + c) * 16384 + i * 1024) = kr[c][i];
            if (c < 5) lds_arrive(lds0 + L_CNT + (C_READY + 2 + c) * 4, 2, lane);
          }
        flag_set(ws, WS_FH + ((qb * 4 + v) * 32 + h) * 8, tag, lane);
        STAMP(11);
      }
    }
    STAMP(4);
    f32x16 acc[4];
    const int wx = (q4 >> 1) * 64;
    bool wo_done = false;
#define WO_DMA() { const char* wsrc_ = wo + (size_t)v * 128 * 1024 + ct * 128;                                   \
      const unsigned wvo_ = (lane >> 3) * 1024 + (((lane & 7) ^ (4 * g1)) << 4);                                 \
      _Pragma("unroll") for (int i = 0; i < 16; ++i) glds16(wsrc_ + i * 8192, wvo_, lds0 + v * 16384 + i * 1024); }
    {
      constexpr int NS = 28, PD = 4;
      const __amdgpu_buffer_rsrc_t rw = __builtin_amdgcn_make_buffer_rsrc((void*)w2, 0, FF * 1024, 0x00020000);
      const unsigned wvo = (lane >> 3) * 1024 + (((lane & 7) ^ (4 * g1)) << 4), wso = v * 448 * 1024 + ct * 128;
      const unsigned ao = WS_A + ((qb * 8 + u * 2) * KSTEPS + 32 + v * 28) * 1024;
      u32x4 wr[PD][2], ar[PD][2];
      const int rot2 = ROT2 ? (3 * ct + 7 * (qb & 3)) % NS : 0;
#define FETCHW(i_, slot) { const int i = (i_) + rot2 - ((i_) + rot2 >= NS ? NS : 0);                             \
        wr[slot][0] = __builtin_amdgcn_raw_buffer_load_b128(rw, wvo, wso + i * 16384, 0); wr[slot][1] = __builtin_amdgcn_raw_buffer_load_b128(rw, wvo, wso + i * 16384 + 8192, 0); }
#define FETCHA(i_, slot) { const int i = (i_) + rot2 - ((i_) + rot2 >= NS ? NS : 0);                             \
        ar[slot][0] = ld_sc1(rs, lane * 16, ao + i * 1024); ar[slot][1] = ld_sc1(rs, lane * 16, ao + (KSTEPS + i) * 1024); }
#pragma unroll
      for (int c2 = 0; c2 < 2; ++c2)
#pragma unroll
        for (int rq = 0; rq < 4; ++rq) {
          u32x2 bw = *(const u32x2*)(b2 + (ct * 64 + c2 * 32 + 8 * rq + 4 * hh) * 2);
          if (v != 0) bw = u32x2{0u, 0u};
          acc[2 * c2][4 * rq] = __uint_as_float(bw[0] << 16); acc[2 * c2][4 * rq + 1] = __uint_as_float(bw[0] & 0xffff0000u);
          acc[2 * c2][4 * rq + 2] = __uint_as_float(bw[1] << 16); acc[2 * c2][4 * rq + 3] = __uint_as_float(bw[1] & 0xffff0000u);
        }
      acc[1] = acc[0]; acc[3] = acc[2];
#pragma unroll
      for (int k = 0; k < PD; ++k) FETCHW(k, k)
      flags_wait(ws, WS_FH + (hh ? (qb * 4 + v) * 32 + 28 : (qb * 4 + u) * 32 + r32) * 8, tag);
      STAMP(5);
      const float zg = __hip_atomic_load((GAS float*)(ws + WS_G) + qb * 256 + v * 64 + lane, RLX_AGENT);
#pragma unroll
      for (int k = 0; k < PD; ++k) FETCHA(k, k)
      *(LAS float*)(L + L_G + (v * 64 + lane) * 4) = zg;
      lds_arrive(lds0 + L_CNT + (C_READY + 7) * 4, 2, lane);
      const lds_ptr wd = L + L_SCR + v * 4096 + lane * 16;
      unsigned ra_ = lds0 + L_SCR + v * 4096 + (8 * hh + q4) * 128 + g1 * 32 + p4 * 8 + wx, rb_ = ra_ ^ 64;
      PIN(ra_); PIN(rb_);
      const lds_cptr ra = (lds_cptr)(uintptr_t)ra_, rb = (lds_cptr)(uintptr_t)rb_;
#define PACE() { SBAR(); if (PSLEEP) __builtin_amdgcn_s_sleep(PSLEEP); SBAR(); }
#define STAGE2(j, wa, wb) { const int buf = ((j) & 1) * 2048;                                                  \
        *(LAS u32x4*)(wd + buf) = wr[j][0]; *(LAS u32x4*)(wd + buf + 1024) = wr[j][1];                          \
        wa = ldtr2(ra + buf, ra + buf + 512);                                                     \
        wb = ldtr2(rb + buf, rb + buf + 512); }
      bf16x8 wa[2], wb[2];
      __builtin_amdgcn_s_setprio(APRIO);
      STAGE2(0, wa[0], wb[0])
#pragma unroll 1
      for (int i0 = 0; i0 < (NO_FFN2 ? 4 : NS); i0 += PD) {
        if (!wo_done && i0 >= 12 && __builtin_amdgcn_readfirstlane(*(const volatile LAS unsigned*)(cnt + C_HALF * 4)) >= 8) { WO_DMA() wo_done = true; }
#pragma unroll
        for (int j = 0; j < PD; ++j) {
          const int in = i0 + PD + j < NS ? i0 + PD + j : NS - 1;
          SBAR();
          if (ABL & 1) { PIN(wr[j][0]); PIN(wr[j][1]); } else FETCHW(in, j)
          STAGE2((j + 1) % PD, wa[(j + 1) & 1], wb[(j + 1) & 1])
          SBAR();
          const bf16x8 a0 = __builtin_bit_cast(bf16x8, ar[j][0]), a1 = __builtin_bit_cast(bf16x8, ar[j][1]);
          acc[0] = MFMA(wa[j & 1], a0, acc[0]); PACE()
          acc[1] = MFMA(wa[j & 1], a1, acc[1]); PACE()
          acc[2] = MFMA(wb[j & 1], a0, acc[2]); PACE()
          acc[3] = MFMA(wb[j & 1], a1, acc[3]);
          SBAR();
          if (ABL & 2) { PIN(ar[j][0]); PIN(ar[j][1]); } else FETCHA(in, j)
        }
      }
    }
    STAMP(6);
    __builtin_amdgcn_s_setprio(3);
    {
    LANE_VARS
    const int wx = (q4 >> 1) * 64;
    if (!wo_done) { lds_wait_slow(cnt + C_HALF * 4, 8); WO_DMA() }
    const int mt = v & 1, kh = v >> 1;
    f32x16 fo[2];
    {
      const lds_ptr xs_ = L + XCH(v) + lane * 16;
      const lds_cptr xr_ = (lds_cptr)L + XCH(v ^ 1) + lane * 16;
      lds_arrive(lds0 + L_CNT + C_X0 * 4, 1, lane);
      lds_wait(cnt + C_X0 * 4, 4);
#pragma unroll
      for (int c2 = 0; c2 < 2; ++c2) {
        const f32x16 snd = mt ? acc[2 * c2] : acc[2 * c2 + 1];
        fo[c2] = mt ? acc[2 * c2 + 1] : acc[2 * c2];
#pragma unroll
        for (int rq = 0; rq < 4; ++rq) *(LAS f32x4*)(xs_ + (c2 * 4 + rq) * 1024) = f32x4{snd[4 * rq], snd[4 * rq + 1], snd[4 * rq + 2], snd[4 * rq + 3]};
      }
      asm volatile("s_waitcnt vmcnt(0)" ::: "memory");
      lds_arrive(lds0 + L_CNT + C_X1 * 4, 1, lane);
      lds_wait(cnt + (C_READY + 7) * 4, 8);
      const float og = bfround(1.0f - gate_from_logit(*(const LAS float*)(L + L_G + (u * 64 + mt * 32 + r32) * 4)));
      lds_wait(cnt + C_X1 * 4, 4);
#pragma unroll
      for (int c2 = 0; c2 < 2; ++c2) {
#pragma unroll
        for (int rq = 0; rq < 4; ++rq) {
          const f32x4 t = *(const LAS f32x4*)(xr_ + (c2 * 4 + rq) * 1024);
#pragma unroll
          for (int r = 0; r < 4; ++r) fo[c2][4 * rq + r] = (fo[c2][4 * rq + r] + t[r]) * og;
        }
        PIN(fo[c2]);
      }
    }
    STAMP(7);
    {
      const lds_cptr rsrc = (lds_cptr)L + kh * 32768 + (4 * hh + q4) * 128 + g1 * 32 + p4 * 8;
      const char* ob = ws + WS_A + (size_t)((qb * 8 + u * 2 + mt) * KSTEPS + kh * 16) * 1024;
      __builtin_amdgcn_s_setprio(0);
      lds_wait_slow(cnt + C_LATE * 4, 4);
      unsigned todo, nx, sp, m, t;
      asm volatile(TAIL_ASM
        : "+{v[32:47]}"(fo[0]), "+{v[48:63]}"(fo[1]), [todo] "=&s"(todo), [nx] "=&s"(nx), [sp] "=&s"(sp), [m] "=&s"(m), [t] "=&s"(t)
        : [la] "v"((unsigned)(uintptr_t)(rsrc + wx)), [lb] "v"((unsigned)(uintptr_t)(rsrc + (wx ^ 64))), [vo] "v"(lane * 16),
          [vp] "v"(r16 * 1024 + 1020), [ob] "s"(ob)
        : TAIL_CLOB, "vcc", "scc", "memory");
    }
    STAMP(9);
    f32x16 fin = kh ? fo[1] : fo[0];
    {
      const f32x16 snd = kh ? fo[0] : fo[1];
#pragma unroll
      for (int rq = 0; rq < 4; ++rq)
        *(LAS f32x4*)(L + XCH(v ^ 1) + rq * 1024 + lane * 16) = f32x4{snd[4 * rq], snd[4 * rq + 1], snd[4 * rq + 2], snd[4 * rq + 3]};
      lds_arrive(lds0 + L_CNT + (C_FIN + mt) * 4, 1, lane);
      lds_wait(cnt + (C_FIN + mt) * 4, 2);
#pragma unroll
      for (int rq = 0; rq < 4; ++rq) {
        const f32x4 t = *(const LAS f32x4*)(L + XCH(v ^ 3) + rq * 1024 + lane * 16);
        fin[4 * rq] += t[0]; fin[4 * rq + 1] += t[1]; fin[4 * rq + 2] += t[2]; fin[4 * rq + 3] += t[3];
      }
    }
    char* op = out + (size_t)(qb * 256 + u * 64 + mt * 32 + r32) * 1024 + (ct * 64 + kh * 32 + 8 * hh) * 2;
#pragma unroll
    for (int pr = 0; pr < 2; ++pr)
      *(u32x4*)(op + pr * 32) = merge32(f32x4{fin[8 * pr], fin[8 * pr + 1], fin[8 * pr + 2], fin[8 * pr + 3]},
                                        f32x4{fin[8 * pr + 4], fin[8 * pr + 5], fin[8 * pr + 6], fin[8 * pr + 7]});
    if (blockIdx.x == 0 && v == 0 && lane == 0) __hip_atomic_store((gu64*)(ws + WS_E), tag, RLX_AGENT);
    }
    STAMP(12);
    STAMP_OUT;
  }
}

extern "C" void kernel_launch(void* const* d_in, const int* in_sizes, int n_in, void* d_out, int out_size,
                              void* d_ws, size_t ws_size, hipStream_t stream) {
  (void)hipFuncSetAttribute((const void*)fused_k, hipFuncAttributeMaxDynamicSharedMemorySize, LDS_BYTES);
  fused_k<<<256, 768, LDS_BYTES, stream>>>((const char*)d_in[0], (const char*)d_in[1], (const char*)d_in[2],
                                           (const char*)d_in[4], (const char*)d_in[5], (const char*)d_in[6],
                                           (const char*)d_in[7], (const char*)d_in[8], (char*)d_ws, (char*)d_out);
}
```

```cpp
#include <hip/hip_runtime.h>
#include <hip/hip_bf16.h>
#include <stdint.h>

typedef __attribute__((ext_vector_type(8))) __bf16 bf16x8;
typedef __attribute__((ext_vector_type(2))) __bf16 bf16x2;
typedef __attribute__((ext_vector_type(4))) short s16x4;
typedef __attribute__((ext_vector_type(8))) short s16x8;
typedef __attribute__((ext_vector_type(16))) float f32x16;
typedef __attribute__((ext_vector_type(4))) float f32x4;
typedef __attribute__((ext_vector_type(4))) unsigned u32x4;
typedef __attribute__((ext_vector_type(2))) unsigned u32x2;
typedef unsigned long long u64;
#define LAS __attribute__((address_space(3)))
#define GAS __attribute__((address_space(1)))
typedef LAS const char* lds_cptr;
typedef LAS char* lds_ptr;
typedef GAS u64 gu64;

#define NO_FFN2 0
#define APRIO 0
#define PSLEEP 0
#define ROT2 0
#define ABL 0
#define GA 3
#define CODEPF 2
#define CODE_LINES 160
#define XORD 1
#define SRC(s) (XORD ? (qb ^ (s)) : ((qb + (s)) & 7))
#define W23 0
#define XR 1
#define ROTA 3
#define HISTEPS 0
#define LATEN 4
#define KARG 1
#define PTW 1
#define LDELAY 6
#define QPRIO 2
constexpr int FF = 1792;
constexpr int KSTEPS = 32 + FF / 16;

constexpr unsigned WS_E = 0;
constexpr unsigned WS_FKV = 4096;
constexpr unsigned WS_FH = WS_FKV + 1024 * 8;
constexpr unsigned WS_FO = WS_FH + 1024 * 8;
constexpr unsigned WS_G = WS_FO + 1024 * 8;
constexpr unsigned WS_KV = 1u << 20;
constexpr unsigned WS_A = WS_KV + 32u * 64 * 2048;
constexpr unsigned WS_END = WS_A + 64u * KSTEPS * 1024;

constexpr unsigned OOB = 0x40000000u;

constexpr int ST_QK = 32768, ST_V = ST_QK + 4096, ST_W1 = ST_V + 2048, ST_BYTES = ST_W1 + 8192;
constexpr int L_SCR = 131072;
constexpr int L_GATE = L_SCR + 16384, L_QX = L_GATE + 1024, L_G = L_QX + 4096, L_E = L_G + 1024, L_CNT = L_E + 16;
constexpr int C_DONE = 0, C_HALF = 1, C_LATE = 2, C_X0 = 3, C_X1 = 4, C_FIN = 5, C_READY = 16;
constexpr int L_ZERO = L_CNT + 28 * 4;
constexpr int LDS_BYTES = L_CNT + 128;

#define STAMP(i)
#define STAMP_DECL
#define STAMP_OUT
#define MFMA(a, b, c) __builtin_amdgcn_mfma_f32_32x32x16_bf16(a, b, c, 0, 0, 0)
#define MFMA16(a, b, c) __builtin_amdgcn_mfma_f32_16x16x32_bf16(a, b, c, 0, 0, 0)
#define INTERLEAVE(n) { _Pragma("unroll") for (int i_ = 0; i_ < (n); ++i_) { __builtin_amdgcn_sched_group_barrier(0x008, 1, 0); __builtin_amdgcn_sched_group_barrier(0x100, 1, 0); } __builtin_amdgcn_sched_group_barrier(0x008, 16, 0); }
#define BAR() asm volatile("s_waitcnt lgkmcnt(0)\n\ts_barrier" ::: "memory")
#define XCH(w, c2) (((c2) ? 65536 : L_SCR) + (w) * 4096)
#define LANE_VARS int lane; asm volatile("v_mbcnt_lo_u32_b32 %0, -1, 0\n\tv_mbcnt_hi_u32_b32 %0, -1, %0" : "=v"(lane)); __builtin_assume(lane >= 0 && lane < 64);                       \
  const int r32 = lane & 31, hh = lane >> 5, g1 = (lane >> 4) & 1, g4 = lane >> 4, r16 = lane & 15, q4 = (lane >> 2) & 3, p4 = lane & 3; \
  (void)r32; (void)hh; (void)g1; (void)g4; (void)r16; (void)q4; (void)p4;
#define DMA_VARS                                                                                                 \
  const unsigned xv0 = (lane >> 3) * 1024 + (((lane & 7) ^ g4) << 4), xv1 = xv0 ^ 64;                            \
  const unsigned qv_ = (lane >> 2) * 3072 + (((p4 >> 1) ^ hh) << 10) + (p4 & 1) * 16;                            \
  const unsigned vv_ = ((r32 >> 1) ^ (4 * g1)) * 3072 + 2048 + (lane & 1) * 16;                                  \
  const unsigned wv0 = (lane >> 3) * (FF * 2) + (((lane & 7) ^ (2 * g1)) << 4), wv1 = wv0 ^ 64;                  \
  (void)qv_; (void)vv_; (void)wv0; (void)wv1;
#define SBAR() __builtin_amdgcn_sched_barrier(0)
#define PIN(x) asm volatile("" : "+v"(x))
#define RLX_AGENT __ATOMIC_RELAXED, __HIP_MEMORY_SCOPE_AGENT
#define SC1 16

__device__ __forceinline__ unsigned cvtpk(float lo, float hi) {
  bf16x2 v; v[0] = (__bf16)lo; v[1] = (__bf16)hi; return __builtin_bit_cast(unsigned, v);
}
__device__ __forceinline__ float bfround(float x) { return (float)(__bf16)x; }
__device__ __forceinline__ bf16x8 ldtr2(lds_cptr p0, lds_cptr p1) {
  s16x4 a = __builtin_amdgcn_ds_read_tr16_b64_v4i16((LAS s16x4*)p0), b = __builtin_amdgcn_ds_read_tr16_b64_v4i16((LAS s16x4*)p1);
  s16x8 r = {a[0], a[1], a[2], a[3], b[0], b[1], b[2], b[3]};
  return __builtin_bit_cast(bf16x8, r);
}
__device__ __forceinline__ bf16x8 ld128(lds_cptr p) { return *(const LAS bf16x8*)p; }
__device__ __forceinline__ void glds16(const char* sbase, unsigned voff, unsigned lds_base) {
  unsigned sv;
  asm volatile("s_mov_b32 %0, m0\n\ts_mov_b32 m0, %3\n\ts_nop 0\n\tglobal_load_lds_dwordx4 %1, %2\n\ts_mov_b32 m0, %0"
               : "=&s"(sv) : "v"(voff), "s"(sbase), "s"(__builtin_amdgcn_readfirstlane(lds_base)) : "memory");
}
__device__ __forceinline__ void st_sc1(u32x4 v, __amdgpu_buffer_rsrc_t rs, unsigned off) { __builtin_amdgcn_raw_buffer_store_b128(v, rs, off, 0, SC1); }
__device__ __forceinline__ float swap_max(float v) {
  auto rr = __builtin_amdgcn_permlane32_swap(__float_as_uint(v), __float_as_uint(v), false, false);
  return fmaxf(__uint_as_float(rr[0]), __uint_as_float(rr[1]));
}
__device__ __forceinline__ u32x4 merge16(f32x4 a, f32x4 b, float s) {
  auto r0 = __builtin_amdgcn_permlane16_swap(cvtpk(a[0] * s, a[1] * s), cvtpk(b[0] * s, b[1] * s), false, false);
  auto r1 = __builtin_amdgcn_permlane16_swap(cvtpk(a[2] * s, a[3] * s), cvtpk(b[2] * s, b[3] * s), false, false);
  return u32x4{r0[0], r1[0], r0[1], r1[1]};
}
__device__ __forceinline__ u32x4 merge32(f32x4 a, f32x4 b) {
  auto r0 = __builtin_amdgcn_permlane32_swap(cvtpk(a[0], a[1]), cvtpk(b[0], b[1]), false, false);
  auto r1 = __builtin_amdgcn_permlane32_swap(cvtpk(a[2], a[3]), cvtpk(b[2], b[3]), false, false);
  return u32x4{r0[0], r1[0], r0[1], r1[1]};
}
__device__ __forceinline__ u32x4 not_empty(u32x4 v) { return u32x4{min(v[0], 0xfffffffeu), min(v[1], 0xfffffffeu), min(v[2], 0xfffffffeu), min(v[3], 0xfffffffeu)}; }
__device__ __forceinline__ unsigned umax4(u32x4 v) { return max(max(v[0], v[1]), max(v[2], v[3])); }
__device__ __forceinline__ u32x4 ld_sc1(__amdgpu_buffer_rsrc_t rs, unsigned voff, unsigned soff) { return __builtin_amdgcn_raw_buffer_load_b128(rs, voff, soff, SC1); }
__device__ __forceinline__ float relu(float x) { return __builtin_amdgcn_fmed3f(x, 0.f, 3.4028235e38f); }
__device__ __forceinline__ float gate_from_logit(float z) {
  z = bfround(z);
  return bfround(1.0f / (1.0f + __expf(-z)));
}
__device__ __forceinline__ float max16(const f32x16& s) {
  float a = fmaxf(fmaxf(s[0], s[1]), s[2]);
#pragma unroll
  for (int r = 3; r < 15; r += 2) a = fmaxf(fmaxf(a, s[r]), s[r + 1]);
  return fmaxf(a, s[15]);
}
__device__ __forceinline__ void pack_p(const f32x16& s, bf16x8& p0, bf16x8& p1) {
  u32x4 a, b;
#pragma unroll
  for (int i = 0; i < 4; ++i) { a[i] = cvtpk(s[2 * i], s[2 * i + 1]); b[i] = cvtpk(s[8 + 2 * i], s[8 + 2 * i + 1]); }
  p0 = __builtin_bit_cast(bf16x8, a); p1 = __builtin_bit_cast(bf16x8, b);
}
__device__ __forceinline__ u64 make_tag(u64 e) {
  const unsigned lo = __builtin_amdgcn_readfirstlane((unsigned)e), hi = __builtin_amdgcn_readfirstlane((unsigned)(e >> 32));
  return (((u64)hi << 32) | lo) * 0x9E3779B97F4A7C15ull + 0xD1B54A32D192ED03ull;
}
__device__ __forceinline__ void lds_arrive(unsigned addr, unsigned n, int lane) {
  asm volatile("s_waitcnt lgkmcnt(0)" ::: "memory");
  if (lane == 0) asm volatile("ds_add_u32 %0, %1" :: "v"(addr), "v"(n) : "memory");
}
constexpr unsigned SPIN_CAP = 1u << 20;
__device__ __forceinline__ void lds_wait(lds_cptr p, unsigned want) {
#pragma unroll 1
  for (unsigned spins = 0; spins < SPIN_CAP; ++spins) {
    const unsigned v = __builtin_amdgcn_readfirstlane(*(const volatile LAS unsigned*)p);
    if (v >= want) break;
    __builtin_amdgcn_s_sleep(1);
  }
  asm volatile("" ::: "memory");
}
__device__ __forceinline__ void lds_wait_slow(lds_cptr p, unsigned want) {
#pragma unroll 1
  for (unsigned spins = 0; spins < (SPIN_CAP >> 3); ++spins) {
    const unsigned v = __builtin_amdgcn_readfirstlane(*(const volatile LAS unsigned*)p);
    if (v >= want) break;
    __builtin_amdgcn_s_sleep(12);
  }
  asm volatile("" ::: "memory");
}
__device__ __forceinline__ void flags_wait(const char* ws, unsigned off, u64 tag) {
  gu64* f = (gu64*)(ws + off);
#pragma unroll 1
  for (unsigned spins = 0; spins < (SPIN_CAP >> 4); ++spins) {
    const bool ok = __hip_atomic_load(f, RLX_AGENT) == tag;
    if (__all(ok)) break;
    __builtin_amdgcn_s_sleep(1);
  }
  asm volatile("s_waitcnt vmcnt(0)" ::: "memory");
}
__device__ __forceinline__ void flag_set(char* ws, unsigned off, u64 tag, int lane) {
  asm volatile("s_waitcnt vmcnt(0)" ::: "memory");
  if (lane == 0) __hip_atomic_store((gu64*)(ws + off), tag, RLX_AGENT);
}

#define ATT_ASM \
  "s_nop 15\n" \
  "Latt_loop%=:\n" \
  "s_waitcnt lgkmcnt(2)\n" \
  "v_mfma_f32_32x32x16_bf16 v[80:95], v[132:135], v[128:131], v[96:111]\n" \
  "ds_read_b128 v[132:135], v152 offset:4096\n" \
  "v_exp_f32 v64, v64\n" \
  "v_exp_f32 v65, v65\n" \
  "v_exp_f32 v66, v66\n" \
  "v_exp_f32 v67, v67\n" \
  "v_exp_f32 v68, v68\n" \
  "s_waitcnt lgkmcnt(1)\n" \
  "v_mfma_f32_32x32x16_bf16 v[112:127], v[136:139], v[144:147], v[112:127]\n" \
  "v_exp_f32 v69, v69\n" \
  "v_exp_f32 v70, v70\n" \
  "v_exp_f32 v71, v71\n" \
  "v_exp_f32 v72, v72\n" \
  "v_exp_f32 v73, v73\n" \
  "v_mfma_f32_32x32x16_bf16 v[112:127], v[140:143], v[148:151], v[112:127]\n" \
  "s_mov_b64 exec, %[msk]\n" \
  "ds_read_b128 v[136:139], v153 offset:0\n" \
  "ds_read_b128 v[140:143], v153 offset:512\n" \
  "s_mov_b64 exec, -1\n" \
  "v_exp_f32 v74, v74\n" \
  "v_exp_f32 v75, v75\n" \
  "v_exp_f32 v76, v76\n" \
  "v_exp_f32 v77, v77\n" \
  "v_exp_f32 v78, v78\n" \
  "v_exp_f32 v79, v79\n" \
  "v_cvt_pk_bf16_f32 v144, v64, v65\n" \
  "v_cvt_pk_bf16_f32 v145, v66, v67\n" \
  "v_cvt_pk_bf16_f32 v146, v68, v69\n" \
  "v_cvt_pk_bf16_f32 v147, v70, v71\n" \
  "v_cvt_pk_bf16_f32 v148, v72, v73\n" \
  "v_cvt_pk_bf16_f32 v149, v74, v75\n" \
  "v_cvt_pk_bf16_f32 v150, v76, v77\n" \
  "v_cvt_pk_bf16_f32 v151, v78, v79\n" \
  "s_waitcnt lgkmcnt(2)\n" \
  "v_mfma_f32_32x32x16_bf16 v[64:79], v[132:135], v[128:131], v[96:111]\n" \
  "ds_read_b128 v[132:135], v152 offset:6144\n" \
  "v_exp_f32 v80, v80\n" \
  "v_exp_f32 v81, v81\n" \
  "v_exp_f32 v82, v82\n" \
  "v_exp_f32 v83, v83\n" \
  "v_exp_f32 v84, v84\n" \
  "s_waitcnt lgkmcnt(1)\n" \
  "v_mfma_f32_32x32x16_bf16 v[112:127], v[136:139], v[144:147], v[112:127]\n" \
  "v_exp_f32 v85, v85\n" \
  "v_exp_f32 v86, v86\n" \
  "v_exp_f32 v87, v87\n" \
  "v_exp_f32 v88, v88\n" \
  "v_exp_f32 v89, v89\n" \
  "v_mfma_f32_32x32x16_bf16 v[112:127], v[140:143], v[148:151], v[112:127]\n" \
  "s_mov_b64 exec, %[msk]\n" \
  "ds_read_b128 v[136:139], v153 offset:2048\n" \
  "ds_read_b128 v[140:143], v153 offset:2560\n" \
  "s_mov_b64 exec, -1\n" \
  "v_exp_f32 v90, v90\n" \
  "v_exp_f32 v91, v91\n" \
  "v_exp_f32 v92, v92\n" \
  "v_exp_f32 v93, v93\n" \
  "v_exp_f32 v94, v94\n" \
  "v_exp_f32 v95, v95\n" \
  "v_cvt_pk_bf16_f32 v144, v80, v81\n" \
  "v_cvt_pk_bf16_f32 v145, v82, v83\n" \
  "v_cvt_pk_bf16_f32 v146, v84, v85\n" \
  "v_cvt_pk_bf16_f32 v147, v86, v87\n" \
  "v_cvt_pk_bf16_f32 v148, v88, v89\n" \
  "v_cvt_pk_bf16_f32 v149, v90, v91\n" \
  "v_cvt_pk_bf16_f32 v150, v92, v93\n" \
  "v_cvt_pk_bf16_f32 v151, v94, v95\n" \
  "s_waitcnt lgkmcnt(2)\n" \
  "v_mfma_f32_32x32x16_bf16 v[80:95], v[132:135], v[128:131], v[96:111]\n" \
  "ds_read_b128 v[132:135], v152 offset:8192\n" \
  "v_exp_f32 v64, v64\n" \
  "v_exp_f32 v65, v65\n" \
  "v_exp_f32 v66, v66\n" \
  "v_exp_f32 v67, v67\n" \
  "v_exp_f32 v68, v68\n" \
  "s_waitcnt lgkmcnt(1)\n" \
  "v_mfma_f32_32x32x16_bf16 v[112:127], v[136:139], v[144:147], v[112:127]\n" \
  "v_exp_f32 v69, v69\n" \
  "v_exp_f32 v70, v70\n" \
  "v_exp_f32 v71, v71\n" \
  "v_exp_f32 v72, v72\n" \
  "v_exp_f32 v73, v73\n" \
  "v_mfma_f32_32x32x16_bf16 v[112:127], v[140:143], v[148:151], v[112:127]\n" \
  "s_mov_b64 exec, %[msk]\n" \
  "ds_read_b128 v[136:139], v153 offset:4096\n" \
  "ds_read_b128 v[140:143], v153 offset:4608\n" \
  "s_mov_b64 exec, -1\n" \
  "v_exp_f32 v74, v74\n" \
  "v_exp_f32 v75, v75\n" \
  "v_exp_f32 v76, v76\n" \
  "v_exp_f32 v77, v77\n" \
  "v_exp_f32 v78, v78\n" \
  "v_exp_f32 v79, v79\n" \
  "v_cvt_pk_bf16_f32 v144, v64, v65\n" \
  "v_cvt_pk_bf16_f32 v145, v66, v67\n" \
  "v_cvt_pk_bf16_f32 v146, v68, v69\n" \
  "v_cvt_pk_bf16_f32 v147, v70, v71\n" \
  "v_cvt_pk_bf16_f32 v148, v72, v73\n" \
  "v_cvt_pk_bf16_f32 v149, v74, v75\n" \
  "v_cvt_pk_bf16_f32 v150, v76, v77\n" \
  "v_cvt_pk_bf16_f32 v151, v78, v79\n" \
  "s_waitcnt lgkmcnt(2)\n" \
  "v_mfma_f32_32x32x16_bf16 v[64:79], v[132:135], v[128:131], v[96:111]\n" \
  "ds_read_b128 v[132:135], v152 offset:10240\n" \
  "v_exp_f32 v80, v80\n" \
  "v_exp_f32 v81, v81\n" \
  "v_exp_f32 v82, v82\n" \
  "v_exp_f32 v83, v83\n" \
  "v_exp_f32 v84, v84\n" \
  "s_waitcnt lgkmcnt(1)\n" \
  "v_mfma_f32_32x32x16_bf16 v[112:127], v[136:139], v[144:147], v[112:127]\n" \
  "v_exp_f32 v85, v85\n" \
  "v_exp_f32 v86, v86\n" \
  "v_exp_f32 v87, v87\n" \
  "v_exp_f32 v88, v88\n" \
  "v_exp_f32 v89, v89\n" \
  "v_mfma_f32_32x32x16_bf16 v[112:127], v[140:143], v[148:151], v[112:127]\n" \
  "s_mov_b64 exec, %[msk]\n" \
  "ds_read_b128 v[136:139], v153 offset:6144\n" \
  "ds_read_b128 v[140:143], v153 offset:6656\n" \
  "s_mov_b64 exec, -1\n" \
  "v_exp_f32 v90, v90\n" \
  "v_exp_f32 v91, v91\n" \
  "v_exp_f32 v92, v92\n" \
  "v_exp_f32 v93, v93\n" \
  "v_exp_f32 v94, v94\n" \
  "v_exp_f32 v95, v95\n" \
  "v_cvt_pk_bf16_f32 v144, v80, v81\n" \
  "v_cvt_pk_bf16_f32 v145, v82, v83\n" \
  "v_cvt_pk_bf16_f32 v146, v84, v85\n" \
  "v_cvt_pk_bf16_f32 v147, v86, v87\n" \
  "v_cvt_pk_bf16_f32 v148, v88, v89\n" \
  "v_cvt_pk_bf16_f32 v149, v90, v91\n" \
  "v_cvt_pk_bf16_f32 v150, v92, v93\n" \
  "v_cvt_pk_bf16_f32 v151, v94, v95\n" \
  "s_waitcnt lgkmcnt(2)\n" \
  "v_mfma_f32_32x32x16_bf16 v[80:95], v[132:135], v[128:131], v[96:111]\n" \
  "ds_read_b128 v[132:135], v152 offset:12288\n" \
  "v_exp_f32 v64, v64\n" \
  "v_exp_f32 v65, v65\n" \
  "v_exp_f32 v66, v66\n" \
  "v_exp_f32 v67, v67\n" \
  "v_exp_f32 v68, v68\n" \
  "s_waitcnt lgkmcnt(1)\n" \
  "v_mfma_f32_32x32x16_bf16 v[112:127], v[136:139], v[144:147], v[112:127]\n" \
  "ds_read_b32 v155, v154\n" \
  "v_exp_f32 v69, v69\n" \
  "v_exp_f32 v70, v70\n" \
  "v_exp_f32 v71, v71\n" \
  "v_exp_f32 v72, v72\n" \
  "v_exp_f32 v73, v73\n" \
  "v_mfma_f32_32x32x16_bf16 v[112:127], v[140:143], v[148:151], v[112:127]\n" \
  "s_mov_b64 exec, %[msk]\n" \
  "ds_read_b128 v[136:139], v153 offset:8192\n" \
  "ds_read_b128 v[140:143], v153 offset:8704\n" \
  "s_mov_b64 exec, -1\n" \
  "v_exp_f32 v74, v74\n" \
  "v_exp_f32 v75, v75\n" \
  "v_exp_f32 v76, v76\n" \
  "v_exp_f32 v77, v77\n" \
  "v_exp_f32 v78, v78\n" \
  "v_exp_f32 v79, v79\n" \
  "v_cvt_pk_bf16_f32 v144, v64, v65\n" \
  "v_cvt_pk_bf16_f32 v145, v66, v67\n" \
  "v_cvt_pk_bf16_f32 v146, v68, v69\n" \
  "v_cvt_pk_bf16_f32 v147, v70, v71\n" \
  "v_cvt_pk_bf16_f32 v148, v72, v73\n" \
  "v_cvt_pk_bf16_f32 v149, v74, v75\n" \
  "v_cvt_pk_bf16_f32 v150, v76, v77\n" \
  "v_cvt_pk_bf16_f32 v151, v78, v79\n" \
  "s_waitcnt lgkmcnt(2)\n" \
  "v_mfma_f32_32x32x16_bf16 v[64:79], v[132:135], v[128:131], v[96:111]\n" \
  "ds_read_b128 v[132:135], v152 offset:14336\n" \
  "v_exp_f32 v80, v80\n" \
  "v_exp_f32 v81, v81\n" \
  "v_exp_f32 v82, v82\n" \
  "v_exp_f32 v83, v83\n" \
  "v_exp_f32 v84, v84\n" \
  "s_waitcnt lgkmcnt(1)\n" \
  "v_mfma_f32_32x32x16_bf16 v[112:127], v[136:139], v[144:147], v[112:127]\n" \
  "v_exp_f32 v85, v85\n" \
  "v_exp_f32 v86, v86\n" \
  "v_exp_f32 v87, v87\n" \
  "v_exp_f32 v88, v88\n" \
  "v_exp_f32 v89, v89\n" \
  "v_mfma_f32_32x32x16_bf16 v[112:127], v[140:143], v[148:151], v[112:127]\n" \
  "s_mov_b64 exec, %[msk]\n" \
  "ds_read_b128 v[136:139], v153 offset:10240\n" \
  "ds_read_b128 v[140:143], v153 offset:10752\n" \
  "s_mov_b64 exec, -1\n" \
  "v_exp_f32 v90, v90\n" \
  "v_exp_f32 v91, v91\n" \
  "v_exp_f32 v92, v92\n" \
  "v_exp_f32 v93, v93\n" \
  "v_exp_f32 v94, v94\n" \
  "v_exp_f32 v95, v95\n" \
  "v_cvt_pk_bf16_f32 v144, v80, v81\n" \
  "v_cvt_pk_bf16_f32 v145, v82, v83\n" \
  "v_cvt_pk_bf16_f32 v146, v84, v85\n" \
  "v_cvt_pk_bf16_f32 v147, v86, v87\n" \
  "v_cvt_pk_bf16_f32 v148, v88, v89\n" \
  "v_cvt_pk_bf16_f32 v149, v90, v91\n" \
  "v_cvt_pk_bf16_f32 v150, v92, v93\n" \
  "v_cvt_pk_bf16_f32 v151, v94, v95\n" \
  "v_readfirstlane_b32 %[tmp], v155\n" \
  "s_cmp_ge_u32 %[tmp], 8\n" \
  "s_cbranch_scc1 Latt_ok%=\n" \
  "s_mov_b32 %[spin], 0x40000\n" \
  "Latt_spin%=:\n" \
  "s_sleep 1\n" \
  "ds_read_b32 v155, v154\n" \
  "s_waitcnt lgkmcnt(0)\n" \
  "v_readfirstlane_b32 %[tmp], v155\n" \
  "s_sub_u32 %[spin], %[spin], 1\n" \
  "s_cmp_eq_u32 %[spin], 0\n" \
  "s_cbranch_scc1 Latt_ok%=\n" \
  "s_cmp_lt_u32 %[tmp], 8\n" \
  "s_cbranch_scc1 Latt_spin%=\n" \
  "Latt_ok%=:\n" \
  "s_waitcnt lgkmcnt(2)\n" \
  "v_mfma_f32_32x32x16_bf16 v[80:95], v[132:135], v[128:131], v[96:111]\n" \
  "ds_read_b128 v[132:135], v152 offset:16384\n" \
  "v_exp_f32 v64, v64\n" \
  "v_exp_f32 v65, v65\n" \
  "v_exp_f32 v66, v66\n" \
  "v_exp_f32 v67, v67\n" \
  "v_exp_f32 v68, v68\n" \
  "s_waitcnt lgkmcnt(1)\n" \
  "v_mfma_f32_32x32x16_bf16 v[112:127], v[136:139], v[144:147], v[112:127]\n" \
  "v_exp_f32 v69, v69\n" \
  "v_exp_f32 v70, v70\n" \
  "v_exp_f32 v71, v71\n" \
  "v_exp_f32 v72, v72\n" \
  "v_exp_f32 v73, v73\n" \
  "v_mfma_f32_32x32x16_bf16 v[112:127], v[140:143], v[148:151], v[112:127]\n" \
  "s_mov_b64 exec, %[msk]\n" \
  "ds_read_b128 v[136:139], v153 offset:12288\n" \
  "ds_read_b128 v[140:143], v153 offset:12800\n" \
  "s_mov_b64 exec, -1\n" \
  "v_exp_f32 v74, v74\n" \
  "v_exp_f32 v75, v75\n" \
  "v_exp_f32 v76, v76\n" \
  "v_exp_f32 v77, v77\n" \
  "v_exp_f32 v78, v78\n" \
  "v_exp_f32 v79, v79\n" \
  "v_cvt_pk_bf16_f32 v144, v64, v65\n" \
  "v_cvt_pk_bf16_f32 v145, v66, v67\n" \
  "v_cvt_pk_bf16_f32 v146, v68, v69\n" \
  "v_cvt_pk_bf16_f32 v147, v70, v71\n" \
  "v_cvt_pk_bf16_f32 v148, v72, v73\n" \
  "v_cvt_pk_bf16_f32 v149, v74, v75\n" \
  "v_cvt_pk_bf16_f32 v150, v76, v77\n" \
  "v_cvt_pk_bf16_f32 v151, v78, v79\n" \
  "s_waitcnt lgkmcnt(2)\n" \
  "v_mfma_f32_32x32x16_bf16 v[64:79], v[132:135], v[128:131], v[96:111]\n" \
  "ds_read_b128 v[132:135], v152 offset:18432\n" \
  "v_exp_f32 v80, v80\n" \
  "v_exp_f32 v81, v81\n" \
  "v_exp_f32 v82, v82\n" \
  "v_exp_f32 v83, v83\n" \
  "v_exp_f32 v84, v84\n" \
  "s_waitcnt lgkmcnt(1)\n" \
  "v_mfma_f32_32x32x16_bf16 v[112:127], v[136:139], v[144:147], v[112:127]\n" \
  "v_exp_f32 v85, v85\n" \
  "v_exp_f32 v86, v86\n" \
  "v_exp_f32 v87, v87\n" \
  "v_exp_f32 v88, v88\n" \
  "v_exp_f32 v89, v89\n" \
  "v_mfma_f32_32x32x16_bf16 v[112:127], v[140:143], v[148:151], v[112:127]\n" \
  "s_mov_b64 exec, %[msk]\n" \
  "ds_read_b128 v[136:139], v153 offset:14336\n" \
  "ds_read_b128 v[140:143], v153 offset:14848\n" \
  "s_mov_b64 exec, -1\n" \
  "v_exp_f32 v90, v90\n" \
  "v_exp_f32 v91, v91\n" \
  "v_exp_f32 v92, v92\n" \
  "v_exp_f32 v93, v93\n" \
  "v_exp_f32 v94, v94\n" \
  "v_exp_f32 v95, v95\n" \
  "v_cvt_pk_bf16_f32 v144, v80, v81\n" \
  "v_cvt_pk_bf16_f32 v145, v82, v83\n" \
  "v_cvt_pk_bf16_f32 v146, v84, v85\n" \
  "v_cvt_pk_bf16_f32 v147, v86, v87\n" \
  "v_cvt_pk_bf16_f32 v148, v88, v89\n" \
  "v_cvt_pk_bf16_f32 v149, v90, v91\n" \
  "v_cvt_pk_bf16_f32 v150, v92, v93\n" \
  "v_cvt_pk_bf16_f32 v151, v94, v95\n" \
  "v_add_u32 v152, 16384, v152\n" \
  "v_add_u32 v153, 16384, v153\n" \
  "v_add_u32 v154, 4, v154\n" \
  "s_sub_u32 %[n], %[n], 1\n" \
  "s_cmp_lg_u32 %[n], 0\n" \
  "s_cbranch_scc1 Latt_loop%=\n" \
  "s_waitcnt lgkmcnt(0)\n" \
  "s_nop 3\n"

#define TAIL_ASM \
  "ds_read_b64_tr_b16 v[128:129], %[la] offset:0\n" \
  "ds_read_b64_tr_b16 v[130:131], %[la] offset:1024\n" \
  "ds_read_b64_tr_b16 v[132:133], %[lb] offset:0\n" \
  "ds_read_b64_tr_b16 v[134:135], %[lb] offset:1024\n" \
  "ds_read_b64_tr_b16 v[136:137], %[la] offset:2048\n" \
  "ds_read_b64_tr_b16 v[138:139], %[la] offset:3072\n" \
  "ds_read_b64_tr_b16 v[140:141], %[lb] offset:2048\n" \
  "ds_read_b64_tr_b16 v[142:143], %[lb] offset:3072\n" \
  "ds_read_b64_tr_b16 v[144:145], %[la] offset:4096\n" \
  "ds_read_b64_tr_b16 v[146:147], %[la] offset:5120\n" \
  "ds_read_b64_tr_b16 v[148:149], %[lb] offset:4096\n" \
  "ds_read_b64_tr_b16 v[150:151], %[lb] offset:5120\n" \
  "ds_read_b64_tr_b16 v[152:153], %[la] offset:6144\n" \
  "ds_read_b64_tr_b16 v[154:155], %[la] offset:7168\n" \
  "ds_read_b64_tr_b16 v[156:157], %[lb] offset:6144\n" \
  "ds_read_b64_tr_b16 v[158:159], %[lb] offset:7168\n" \
  "v_add_u32 v161, 0x1000, %[vo]\n" \
  "v_add_u32 v162, 0x2000, %[vo]\n" \
  "v_add_u32 v163, 0x3000, %[vo]\n" \
  "v_mov_b32 v64, -1\n" \
  "v_mov_b32 v68, -1\n" \
  "v_mov_b32 v72, -1\n" \
  "v_mov_b32 v76, -1\n" \
  "v_mov_b32 v80, -1\n" \
  "v_mov_b32 v84, -1\n" \
  "v_mov_b32 v88, -1\n" \
  "v_mov_b32 v92, -1\n" \
  "v_mov_b32 v96, -1\n" \
  "v_mov_b32 v100, -1\n" \
  "v_mov_b32 v104, -1\n" \
  "v_mov_b32 v108, -1\n" \
  "v_mov_b32 v112, -1\n" \
  "v_mov_b32 v116, -1\n" \
  "v_mov_b32 v120, -1\n" \
  "v_mov_b32 v124, -1\n" \
  "s_mov_b32 %[todo], 0xffff\n" \
  "s_mov_b32 %[nx], 0\n" \
  "s_mov_b32 %[sp], 0x4000\n" \
  "s_mov_b32 %[thr], 3\n" \
  "Lt_loop%=:\n" \
  "s_mov_b32 %[m], %[todo]\n" \
  "s_bcnt1_i32_b32 %[t], %[todo]\n" \
  "s_cmp_le_u32 %[t], %[thr]\n" \
  "s_cbranch_scc1 Lt_fetch%=\n" \
  "global_load_dword v160, %[vp], %[ob] sc1\n" \
  "ds_read_b32 v164, %[ce]\n" \
  "s_waitcnt lgkmcnt(0)\n" \
  "v_readfirstlane_b32 %[t], v164\n" \
  "s_cmp_ge_u32 %[t], 16\n" \
  "s_cselect_b32 %[thr], 16, 3\n" \
  "s_waitcnt vmcnt(0)\n" \
  "v_cmp_ne_u32 vcc, -1, v160\n" \
  "s_nop 1\n" \
  "s_and_b32 %[m], %[m], vcc_lo\n" \
  "s_bcnt1_i32_b32 %[t], %[m]\n" \
  "s_cmp_ge_u32 %[t], 4\n" \
  "s_cbranch_scc1 Lt_fetch%=\n" \
  "s_cmp_eq_u32 %[thr], 16\n" \
  "s_cbranch_scc1 Lt_next%=\n" \
  "s_sleep 3\n" \
  "s_branch Lt_next%=\n" \
  "Lt_fetch%=:\n" \
  "s_bitcmp1_b32 %[m], 0\n" \
  "s_cbranch_scc0 Lt_s0_%=\n" \
  "global_load_dwordx4 v[64:67], %[vo], %[ob] offset:0 sc1\n" \
  "Lt_s0_%=:\n" \
  "s_bitcmp1_b32 %[m], 1\n" \
  "s_cbranch_scc0 Lt_s1_%=\n" \
  "global_load_dwordx4 v[68:71], %[vo], %[ob] offset:1024 sc1\n" \
  "Lt_s1_%=:\n" \
  "s_bitcmp1_b32 %[m], 2\n" \
  "s_cbranch_scc0 Lt_s2_%=\n" \
  "global_load_dwordx4 v[72:75], %[vo], %[ob] offset:2048 sc1\n" \
  "Lt_s2_%=:\n" \
  "s_bitcmp1_b32 %[m], 3\n" \
  "s_cbranch_scc0 Lt_s3_%=\n" \
  "global_load_dwordx4 v[76:79], %[vo], %[ob] offset:3072 sc1\n" \
  "Lt_s3_%=:\n" \
  "s_bitcmp1_b32 %[m], 4\n" \
  "s_cbranch_scc0 Lt_s4_%=\n" \
  "global_load_dwordx4 v[80:83], v161, %[ob] offset:0 sc1\n" \
  "Lt_s4_%=:\n" \
  "s_bitcmp1_b32 %[m], 5\n" \
  "s_cbranch_scc0 Lt_s5_%=\n" \
  "global_load_dwordx4 v[84:87], v161, %[ob] offset:1024 sc1\n" \
  "Lt_s5_%=:\n" \
  "s_bitcmp1_b32 %[m], 6\n" \
  "s_cbranch_scc0 Lt_s6_%=\n" \
  "global_load_dwordx4 v[88:91], v161, %[ob] offset:2048 sc1\n" \
  "Lt_s6_%=:\n" \
  "s_bitcmp1_b32 %[m], 7\n" \
  "s_cbranch_scc0 Lt_s7_%=\n" \
  "global_load_dwordx4 v[92:95], v161, %[ob] offset:3072 sc1\n" \
  "Lt_s7_%=:\n" \
  "s_bitcmp1_b32 %[m], 8\n" \
  "s_cbranch_scc0 Lt_s8_%=\n" \
  "global_load_dwordx4 v[96:99], v162, %[ob] offset:0 sc1\n" \
  "Lt_s8_%=:\n" \
  "s_bitcmp1_b32 %[m], 9\n" \
  "s_cbranch_scc0 Lt_s9_%=\n" \
  "global_load_dwordx4 v[100:103], v162, %[ob] offset:1024 sc1\n" \
  "Lt_s9_%=:\n" \
  "s_bitcmp1_b32 %[m], 10\n" \
  "s_cbranch_scc0 Lt_s10_%=\n" \
  "global_load_dwordx4 v[104:107], v162, %[ob] offset:2048 sc1\n" \
  "Lt_s10_%=:\n" \
  "s_bitcmp1_b32 %[m], 11\n" \
  "s_cbranch_scc0 Lt_s11_%=\n" \
  "global_load_dwordx4 v[108:111], v162, %[ob] offset:3072 sc1\n" \
  "Lt_s11_%=:\n" \
  "s_bitcmp1_b32 %[m], 12\n" \
  "s_cbranch_scc0 Lt_s12_%=\n" \
  "global_load_dwordx4 v[112:115], v163, %[ob] offset:0 sc1\n" \
  "Lt_s12_%=:\n" \
  "s_bitcmp1_b32 %[m], 13\n" \
  "s_cbranch_scc0 Lt_s13_%=\n" \
  "global_load_dwordx4 v[116:119], v163, %[ob] offset:1024 sc1\n" \
  "Lt_s13_%=:\n" \
  "s_bitcmp1_b32 %[m], 14\n" \
  "s_cbranch_scc0 Lt_s14_%=\n" \
  "global_load_dwordx4 v[120:123], v163, %[ob] offset:2048 sc1\n" \
  "Lt_s14_%=:\n" \
  "s_bitcmp1_b32 %[m], 15\n" \
  "s_cbranch_scc0 Lt_s15_%=\n" \
  "global_load_dwordx4 v[124:127], v163, %[ob] offset:3072 sc1\n" \
  "Lt_s15_%=:\n" \
  "s_getpc_b64 s[68:69]\n" \
  "Lt_pc%=:\n" \
  "s_mul_i32 %[t], %[nx], 60\n" \
  "s_add_u32 %[t], %[t], Lt_chain%=-Lt_pc%=\n" \
  "s_waitcnt vmcnt(0)\n" \
  "v_max3_u32 v164, v64, v65, v66\n" \
  "v_max_u32 v164, v164, v67\n" \
  "v_cmp_eq_u32 s[36:37], -1, v164\n" \
  "v_max3_u32 v165, v68, v69, v70\n" \
  "v_max_u32 v165, v165, v71\n" \
  "v_cmp_eq_u32 s[38:39], -1, v165\n" \
  "v_max3_u32 v164, v72, v73, v74\n" \
  "v_max_u32 v164, v164, v75\n" \
  "v_cmp_eq_u32 s[40:41], -1, v164\n" \
  "v_max3_u32 v165, v76, v77, v78\n" \
  "v_max_u32 v165, v165, v79\n" \
  "v_cmp_eq_u32 s[42:43], -1, v165\n" \
  "v_max3_u32 v164, v80, v81, v82\n" \
  "v_max_u32 v164, v164, v83\n" \
  "v_cmp_eq_u32 s[44:45], -1, v164\n" \
  "v_max3_u32 v165, v84, v85, v86\n" \
  "v_max_u32 v165, v165, v87\n" \
  "v_cmp_eq_u32 s[46:47], -1, v165\n" \
  "v_max3_u32 v164, v88, v89, v90\n" \
  "v_max_u32 v164, v164, v91\n" \
  "v_cmp_eq_u32 s[48:49], -1, v164\n" \
  "v_max3_u32 v165, v92, v93, v94\n" \
  "v_max_u32 v165, v165, v95\n" \
  "v_cmp_eq_u32 s[50:51], -1, v165\n" \
  "v_max3_u32 v164, v96, v97, v98\n" \
  "v_max_u32 v164, v164, v99\n" \
  "v_cmp_eq_u32 s[52:53], -1, v164\n" \
  "v_max3_u32 v165, v100, v101, v102\n" \
  "v_max_u32 v165, v165, v103\n" \
  "v_cmp_eq_u32 s[54:55], -1, v165\n" \
  "v_max3_u32 v164, v104, v105, v106\n" \
  "v_max_u32 v164, v164, v107\n" \
  "v_cmp_eq_u32 s[56:57], -1, v164\n" \
  "v_max3_u32 v165, v108, v109, v110\n" \
  "v_max_u32 v165, v165, v111\n" \
  "v_cmp_eq_u32 s[58:59], -1, v165\n" \
  "v_max3_u32 v164, v112, v113, v114\n" \
  "v_max_u32 v164, v164, v115\n" \
  "v_cmp_eq_u32 s[60:61], -1, v164\n" \
  "v_max3_u32 v165, v116, v117, v118\n" \
  "v_max_u32 v165, v165, v119\n" \
  "v_cmp_eq_u32 s[62:63], -1, v165\n" \
  "v_max3_u32 v164, v120, v121, v122\n" \
  "v_max_u32 v164, v164, v123\n" \
  "v_cmp_eq_u32 s[64:65], -1, v164\n" \
  "v_max3_u32 v165, v124, v125, v126\n" \
  "v_max_u32 v165, v165, v127\n" \
  "v_cmp_eq_u32 s[66:67], -1, v165\n" \
  "s_add_u32 s68, s68, %[t]\n" \
  "s_addc_u32 s69, s69, 0\n" \
  "s_mov_b32 %[todo], 1\n" \
  "s_cmp_lg_u64 s[66:67], 0\n" \
  "s_addc_u32 %[todo], %[todo], %[todo]\n" \
  "s_cmp_lg_u64 s[64:65], 0\n" \
  "s_addc_u32 %[todo], %[todo], %[todo]\n" \
  "s_cmp_lg_u64 s[62:63], 0\n" \
  "s_addc_u32 %[todo], %[todo], %[todo]\n" \
  "s_cmp_lg_u64 s[60:61], 0\n" \
  "s_addc_u32 %[todo], %[todo], %[todo]\n" \
  "s_cmp_lg_u64 s[58:59], 0\n" \
  "s_addc_u32 %[todo], %[todo], %[todo]\n" \
  "s_cmp_lg_u64 s[56:57], 0\n" \
  "s_addc_u32 %[todo], %[todo], %[todo]\n" \
  "s_cmp_lg_u64 s[54:55], 0\n" \
  "s_addc_u32 %[todo], %[todo], %[todo]\n" \
  "s_cmp_lg_u64 s[52:53], 0\n" \
  "s_addc_u32 %[todo], %[todo], %[todo]\n" \
  "s_cmp_lg_u64 s[50:51], 0\n" \
  "s_addc_u32 %[todo], %[todo], %[todo]\n" \
  "s_cmp_lg_u64 s[48:49], 0\n" \
  "s_addc_u32 %[todo], %[todo], %[todo]\n" \
  "s_cmp_lg_u64 s[46:47], 0\n" \
  "s_addc_u32 %[todo], %[todo], %[todo]\n" \
  "s_cmp_lg_u64 s[44:45], 0\n" \
  "s_addc_u32 %[todo], %[todo], %[todo]\n" \
  "s_cmp_lg_u64 s[42:43], 0\n" \
  "s_addc_u32 %[todo], %[todo], %[todo]\n" \
  "s_cmp_lg_u64 s[40:41], 0\n" \
  "s_addc_u32 %[todo], %[todo], %[todo]\n" \
  "s_cmp_lg_u64 s[38:39], 0\n" \
  "s_addc_u32 %[todo], %[todo], %[todo]\n" \
  "s_cmp_lg_u64 s[36:37], 0\n" \
  "s_addc_u32 %[todo], %[todo], %[todo]\n" \
  "s_ff1_i32_b32 %[nx], %[todo]\n" \
  "s_and_b32 %[todo], %[todo], 0xffff\n" \
  "s_setpc_b64 s[68:69]\n" \
  "Lt_chain%=:\n" \
  "s_cmp_eq_u32 %[nx], 0\n" \
  "s_cbranch_scc1 Lt_next%=\n" \
  "s_waitcnt lgkmcnt(12)\n" \
  "v_mfma_f32_32x32x16_bf16 v[32:47], v[128:131], v[64:67], v[32:47]\n" \
  "v_mfma_f32_32x32x16_bf16 v[48:63], v[132:135], v[64:67], v[48:63]\n" \
  "ds_read_b64_tr_b16 v[128:129], %[la] offset:8192\n" \
  "ds_read_b64_tr_b16 v[130:131], %[la] offset:9216\n" \
  "ds_read_b64_tr_b16 v[132:133], %[lb] offset:8192\n" \
  "ds_read_b64_tr_b16 v[134:135], %[lb] offset:9216\n" \
  "s_cmp_eq_u32 %[nx], 1\n" \
  "s_cbranch_scc1 Lt_next%=\n" \
  "s_waitcnt lgkmcnt(12)\n" \
  "v_mfma_f32_32x32x16_bf16 v[32:47], v[136:139], v[68:71], v[32:47]\n" \
  "v_mfma_f32_32x32x16_bf16 v[48:63], v[140:143], v[68:71], v[48:63]\n" \
  "ds_read_b64_tr_b16 v[136:137], %[la] offset:10240\n" \
  "ds_read_b64_tr_b16 v[138:139], %[la] offset:11264\n" \
  "ds_read_b64_tr_b16 v[140:141], %[lb] offset:10240\n" \
  "ds_read_b64_tr_b16 v[142:143], %[lb] offset:11264\n" \
  "s_cmp_eq_u32 %[nx], 2\n" \
  "s_cbranch_scc1 Lt_next%=\n" \
  "s_waitcnt lgkmcnt(12)\n" \
  "v_mfma_f32_32x32x16_bf16 v[32:47], v[144:147], v[72:75], v[32:47]\n" \
  "v_mfma_f32_32x32x16_bf16 v[48:63], v[148:151], v[72:75], v[48:63]\n" \
  "ds_read_b64_tr_b16 v[144:145], %[la] offset:12288\n" \
  "ds_read_b64_tr_b16 v[146:147], %[la] offset:13312\n" \
  "ds_read_b64_tr_b16 v[148:149], %[lb] offset:12288\n" \
  "ds_read_b64_tr_b16 v[150:151], %[lb] offset:13312\n" \
  "s_cmp_eq_u32 %[nx], 3\n" \
  "s_cbranch_scc1 Lt_next%=\n" \
  "s_waitcnt lgkmcnt(12)\n" \
  "v_mfma_f32_32x32x16_bf16 v[32:47], v[152:155], v[76:79], v[32:47]\n" \
  "v_mfma_f32_32x32x16_bf16 v[48:63], v[156:159], v[76:79], v[48:63]\n" \
  "ds_read_b64_tr_b16 v[152:153], %[la] offset:14336\n" \
  "ds_read_b64_tr_b16 v[154:155], %[la] offset:15360\n" \
  "ds_read_b64_tr_b16 v[156:157], %[lb] offset:14336\n" \
  "ds_read_b64_tr_b16 v[158:159], %[lb] offset:15360\n" \
  "s_cmp_eq_u32 %[nx], 4\n" \
  "s_cbranch_scc1 Lt_next%=\n" \
  "s_waitcnt lgkmcnt(12)\n" \
  "v_mfma_f32_32x32x16_bf16 v[32:47], v[128:131], v[80:83], v[32:47]\n" \
  "v_mfma_f32_32x32x16_bf16 v[48:63], v[132:135], v[80:83], v[48:63]\n" \
  "ds_read_b64_tr_b16 v[128:129], %[la] offset:16384\n" \
  "ds_read_b64_tr_b16 v[130:131], %[la] offset:17408\n" \
  "ds_read_b64_tr_b16 v[132:133], %[lb] offset:16384\n" \
  "ds_read_b64_tr_b16 v[134:135], %[lb] offset:17408\n" \
  "s_cmp_eq_u32 %[nx], 5\n" \
  "s_cbranch_scc1 Lt_next%=\n" \
  "s_waitcnt lgkmcnt(12)\n" \
  "v_mfma_f32_32x32x16_bf16 v[32:47], v[136:139], v[84:87], v[32:47]\n" \
  "v_mfma_f32_32x32x16_bf16 v[48:63], v[140:143], v[84:87], v[48:63]\n" \
  "ds_read_b64_tr_b16 v[136:137], %[la] offset:18432\n" \
  "ds_read_b64_tr_b16 v[138:139], %[la] offset:19456\n" \
  "ds_read_b64_tr_b16 v[140:141], %[lb] offset:18432\n" \
  "ds_read_b64_tr_b16 v[142:143], %[lb] offset:19456\n" \
  "s_cmp_eq_u32 %[nx], 6\n" \
  "s_cbranch_scc1 Lt_next%=\n" \
  "s_waitcnt lgkmcnt(12)\n" \
  "v_mfma_f32_32x32x16_bf16 v[32:47], v[144:147], v[88:91], v[32:47]\n" \
  "v_mfma_f32_32x32x16_bf16 v[48:63], v[148:151], v[88:91], v[48:63]\n" \
  "ds_read_b64_tr_b16 v[144:145], %[la] offset:20480\n" \
  "ds_read_b64_tr_b16 v[146:147], %[la] offset:21504\n" \
  "ds_read_b64_tr_b16 v[148:149], %[lb] offset:20480\n" \
  "ds_read_b64_tr_b16 v[150:151], %[lb] offset:21504\n" \
  "s_cmp_eq_u32 %[nx], 7\n" \
  "s_cbranch_scc1 Lt_next%=\n" \
  "s_waitcnt lgkmcnt(12)\n" \
  "v_mfma_f32_32x32x16_bf16 v[32:47], v[152:155], v[92:95], v[32:47]\n" \
  "v_mfma_f32_32x32x16_bf16 v[48:63], v[156:159], v[92:95], v[48:63]\n" \
  "ds_read_b64_tr_b16 v[152:153], %[la] offset:22528\n" \
  "ds_read_b64_tr_b16 v[154:155], %[la] offset:23552\n" \
  "ds_read_b64_tr_b16 v[156:157], %[lb] offset:22528\n" \
  "ds_read_b64_tr_b16 v[158:159], %[lb] offset:23552\n" \
  "s_cmp_eq_u32 %[nx], 8\n" \
  "s_cbranch_scc1 Lt_next%=\n" \
  "s_waitcnt lgkmcnt(12)\n" \
  "v_mfma_f32_32x32x16_bf16 v[32:47], v[128:131], v[96:99], v[32:47]\n" \
  "v_mfma_f32_32x32x16_bf16 v[48:63], v[132:135], v[96:99], v[48:63]\n" \
  "ds_read_b64_tr_b16 v[128:129], %[la] offset:24576\n" \
  "ds_read_b64_tr_b16 v[130:131], %[la] offset:25600\n" \
  "ds_read_b64_tr_b16 v[132:133], %[lb] offset:24576\n" \
  "ds_read_b64_tr_b16 v[134:135], %[lb] offset:25600\n" \
  "s_cmp_eq_u32 %[nx], 9\n" \
  "s_cbranch_scc1 Lt_next%=\n" \
  "s_waitcnt lgkmcnt(12)\n" \
  "v_mfma_f32_32x32x16_bf16 v[32:47], v[136:139], v[100:103], v[32:47]\n" \
  "v_mfma_f32_32x32x16_bf16 v[48:63], v[140:143], v[100:103], v[48:63]\n" \
  "ds_read_b64_tr_b16 v[136:137], %[la] offset:26624\n" \
  "ds_read_b64_tr_b16 v[138:139], %[la] offset:27648\n" \
  "ds_read_b64_tr_b16 v[140:141], %[lb] offset:26624\n" \
  "ds_read_b64_tr_b16 v[142:143], %[lb] offset:27648\n" \
  "s_cmp_eq_u32 %[nx], 10\n" \
  "s_cbranch_scc1 Lt_next%=\n" \
  "s_waitcnt lgkmcnt(12)\n" \
  "v_mfma_f32_32x32x16_bf16 v[32:47], v[144:147], v[104:107], v[32:47]\n" \
  "v_mfma_f32_32x32x16_bf16 v[48:63], v[148:151], v[104:107], v[48:63]\n" \
  "ds_read_b64_tr_b16 v[144:145], %[la] offset:28672\n" \
  "ds_read_b64_tr_b16 v[146:147], %[la] offset:29696\n" \
  "ds_read_b64_tr_b16 v[148:149], %[lb] offset:28672\n" \
  "ds_read_b64_tr_b16 v[150:151], %[lb] offset:29696\n" \
  "s_cmp_eq_u32 %[nx], 11\n" \
  "s_cbranch_scc1 Lt_next%=\n" \
  "s_waitcnt lgkmcnt(12)\n" \
  "v_mfma_f32_32x32x16_bf16 v[32:47], v[152:155], v[108:111], v[32:47]\n" \
  "v_mfma_f32_32x32x16_bf16 v[48:63], v[156:159], v[108:111], v[48:63]\n" \
  "ds_read_b64_tr_b16 v[152:153], %[la] offset:30720\n" \
  "ds_read_b64_tr_b16 v[154:155], %[la] offset:31744\n" \
  "ds_read_b64_tr_b16 v[156:157], %[lb] offset:30720\n" \
  "ds_read_b64_tr_b16 v[158:159], %[lb] offset:31744\n" \
  "s_cmp_eq_u32 %[nx], 12\n" \
  "s_cbranch_scc1 Lt_next%=\n" \
  "s_waitcnt lgkmcnt(12)\n" \
  "v_mfma_f32_32x32x16_bf16 v[32:47], v[128:131], v[112:115], v[32:47]\n" \
  "v_mfma_f32_32x32x16_bf16 v[48:63], v[132:135], v[112:115], v[48:63]\n" \
  "ds_read_b64_tr_b16 v[128:129], %[la] offset:32768\n" \
  "ds_read_b64_tr_b16 v[130:131], %[la] offset:33792\n" \
  "ds_read_b64_tr_b16 v[132:133], %[lb] offset:32768\n" \
  "ds_read_b64_tr_b16 v[134:135], %[lb] offset:33792\n" \
  "s_cmp_eq_u32 %[nx], 13\n" \
  "s_cbranch_scc1 Lt_next%=\n" \
  "s_waitcnt lgkmcnt(12)\n" \
  "v_mfma_f32_32x32x16_bf16 v[32:47], v[136:139], v[116:119], v[32:47]\n" \
  "v_mfma_f32_32x32x16_bf16 v[48:63], v[140:143], v[116:119], v[48:63]\n" \
  "ds_read_b64_tr_b16 v[136:137], %[la] offset:34816\n" \
  "ds_read_b64_tr_b16 v[138:139], %[la] offset:35840\n" \
  "ds_read_b64_tr_b16 v[140:141], %[lb] offset:34816\n" \
  "ds_read_b64_tr_b16 v[142:143], %[lb] offset:35840\n" \
  "s_cmp_eq_u32 %[nx], 14\n" \
  "s_cbranch_scc1 Lt_next%=\n" \
  "s_waitcnt lgkmcnt(12)\n" \
  "v_mfma_f32_32x32x16_bf16 v[32:47], v[144:147], v[120:123], v[32:47]\n" \
  "v_mfma_f32_32x32x16_bf16 v[48:63], v[148:151], v[120:123], v[48:63]\n" \
  "ds_read_b64_tr_b16 v[144:145], %[la] offset:36864\n" \
  "ds_read_b64_tr_b16 v[146:147], %[la] offset:37888\n" \
  "ds_read_b64_tr_b16 v[148:149], %[lb] offset:36864\n" \
  "ds_read_b64_tr_b16 v[150:151], %[lb] offset:37888\n" \
  "s_cmp_eq_u32 %[nx], 15\n" \
  "s_cbranch_scc1 Lt_next%=\n" \
  "s_waitcnt lgkmcnt(12)\n" \
  "v_mfma_f32_32x32x16_bf16 v[32:47], v[152:155], v[124:127], v[32:47]\n" \
  "v_mfma_f32_32x32x16_bf16 v[48:63], v[156:159], v[124:127], v[48:63]\n" \
  "ds_read_b64_tr_b16 v[152:153], %[la] offset:38912\n" \
  "ds_read_b64_tr_b16 v[154:155], %[la] offset:39936\n" \
  "ds_read_b64_tr_b16 v[156:157], %[lb] offset:38912\n" \
  "ds_read_b64_tr_b16 v[158:159], %[lb] offset:39936\n" \
  "s_branch Lt_done%=\n" \
  "Lt_next%=:\n" \
  "s_sub_u32 %[sp], %[sp], 1\n" \
  "s_cmp_lg_u32 %[sp], 0\n" \
  "s_cbranch_scc1 Lt_loop%=\n" \
  "Lt_done%=:\n" \
  "s_waitcnt lgkmcnt(0)\n" \
  "s_nop 7\n"
#define TAIL_CLOB "v64", "v65", "v66", "v67", "v68", "v69", "v70", "v71", "v72", "v73", "v74", "v75", "v76", "v77", "v78", "v79", "v80", "v81", "v82", "v83", "v84", "v85", "v86", "v87", "v88", "v89", "v90", "v91", "v92", "v93", "v94", "v95", "v96", "v97", "v98", "v99", "v100", "v101", "v102", "v103", "v104", "v105", "v106", "v107", "v108", "v109", "v110", "v111", "v112", "v113", "v114", "v115", "v116", "v117", "v118", "v119", "v120", "v121", "v122", "v123", "v124", "v125", "v126", "v127", "v128", "v129", "v130", "v131", "v132", "v133", "v134", "v135", "v136", "v137", "v138", "v139", "v140", "v141", "v142", "v143", "v144", "v145", "v146", "v147", "v148", "v149", "v150", "v151", "v152", "v153", "v154", "v155", "v156", "v157", "v158", "v159", "v160", "v161", "v162", "v163", "v164", "v165", "s36", "s37", "s38", "s39", "s40", "s41", "s42", "s43", "s44", "s45", "s46", "s47", "s48", "s49", "s50", "s51", "s52", "s53", "s54", "s55", "s56", "s57", "s58", "s59", "s60", "s61", "s62", "s63", "s64", "s65", "s66", "s67", "s68", "s69"

__global__ __launch_bounds__(768, 3) void fused_k(const char* __restrict__ x, const char* __restrict__ wqkv,
                                                  const char* __restrict__ wo, const char* __restrict__ w1,
                                                  const char* __restrict__ b1, const char* __restrict__ w2,
                                                  const char* __restrict__ b2, const char* __restrict__ gw,
                                                  char* ws, char* __restrict__ out) {
  extern __shared__ __attribute__((aligned(16))) char smem[];
  const int tid = threadIdx.x;
  const int wid = __builtin_amdgcn_readfirstlane(tid >> 6);
  const int xg = blockIdx.x & 7, ig = blockIdx.x >> 3;
  const int qb = (xg & 1) * 4 + (ig & 3), h = (xg >> 1) * 8 + (ig >> 2);
  const bool ffn = (h & 7) < 7;
  const int hf = (h >> 3) * 7 + (ffn ? h & 7 : 6);
  const unsigned lds0 = __builtin_amdgcn_readfirstlane((unsigned)(uintptr_t)smem);
  lds_ptr L = (lds_ptr)smem;
  const __amdgpu_buffer_rsrc_t rs = __builtin_amdgcn_make_buffer_rsrc(ws, 0, WS_END, 0x00020000);
  const int u = h >> 3, ct = h & 7;
  const lds_cptr cnt = (lds_cptr)L + L_CNT;
  u64 pc; asm volatile("s_getpc_b64 %0" : "=s"(pc));
  asm volatile("" :: "s"(x), "s"(wqkv), "s"(w1), "s"(b1), "s"(gw), "s"(ws));
  STAMP_DECL
  STAMP(0);
  if (tid < 32) *(LAS unsigned*)(L + L_CNT + tid * 4) = tid == C_READY + 8 ? 8u : 0u;

  const int j = wid & 3;
  const int rot = ROTA == 1 ? (h + 2 * qb) & 7 : (ROTA == 2 ? ((h & 1) * 4 + (qb & 1) * 2) : (ROTA == 3 ? (h + qb) & 1 : (ROTA == 4 ? (h + qb) & 3 : (ROTA == 5 ? ((h + qb) & 1) * 2 : (ROTA == 6 ? (h + qb) % 3 : (ROTA == 7 ? ((h + qb) & 3) == 0 : 0))))));
#define RC(c) (XR ? ((c) ^ rot) : (((c) + rot) & 7))
  const char* xs = x + (size_t)(qb * 256 + j * 64) * 1024;
  const char* wq = wqkv + h * 32 + (size_t)j * 16 * 3072;
  const char* ww = w1 + hf * 128 + (size_t)j * 16 * (FF * 2);
#define DMA_X(c, sb, i0) { _Pragma("unroll") for (int i = (i0); i < (i0) + 4; ++i)                              \
      glds16(xs + RC(c) * 128 + i * 8192, (i & 1) ? xv1 : xv0, (sb) + (j * 8 + i) * 1024); }
#define DMA_QKV(c, sb) { glds16(wq + (size_t)RC(c) * 64 * 3072, qv_, (sb) + ST_QK + j * 1024);                    \
      if (lane < 32) glds16(wq + (size_t)RC(c) * 64 * 3072, vv_, (sb) + ST_V + j * 512); }
#define DMA_W1(c, sb) { glds16(ww + (size_t)RC(c) * 64 * (FF * 2), wv0, (sb) + ST_W1 + j * 2048);                 \
      glds16(ww + (size_t)(RC(c) * 64 + 8) * (FF * 2), wv1, (sb) + ST_W1 + j * 2048 + 1024); }

  if (wid < 8) {
    LANE_VARS DMA_VARS
    bf16x8 qf;
    int tile;
    if (wid >= 4) {
      __builtin_amdgcn_s_setprio(3);
      __builtin_amdgcn_s_sleep(LDELAY);
      unsigned so = 2 * ST_BYTES;
#pragma unroll 1
      for (int c = 1; c < 8; ++c) {
        const unsigned sb = lds0 + so;
        DMA_QKV(c, sb) DMA_X(c, sb, 0)
        asm volatile("s_waitcnt vmcnt(6)\n\ts_barrier" ::: "memory");
        DMA_X(c, sb, 4) DMA_W1(c, sb)
        so = so == 2 * ST_BYTES ? 0 : so + ST_BYTES;
      }
      asm volatile("s_waitcnt vmcnt(0)\n\ts_barrier" ::: "memory");
      STAMP(1);
      tile = qb * 8 + 2 * j + 1;
    } else {
      DMA_QKV(0, lds0 + ST_BYTES) DMA_X(0, lds0 + ST_BYTES, 0)
      __builtin_amdgcn_s_setprio(QPRIO);
      if (wid == 0 && !ffn) *(LAS u32x4*)(L + L_GATE + lane * 16) = *(const u32x4*)(gw + (XR ? (lane ^ (rot * 8)) : ((lane + rot * 8) & 63)) * 16);
      if (wid == 2 && lane < CODE_LINES / 32 * CODEPF) {
        const unsigned cw = *(const GAS unsigned*)((pc & ~127ull) + (((ig * (CODE_LINES / 32)) + lane) % CODE_LINES) * 128);
        asm volatile("" :: "v"(cw));
      }
      if (wid == 1 && lane == 0) *(LAS u64*)(L + L_E) = __hip_atomic_load((gu64*)(ws + WS_E), RLX_AGENT);
      const unsigned xo0 = (wid * 64 + r16) * 128 + ((g4 ^ (r16 >> 1)) << 4), xo1 = (wid * 64 + r16) * 128 + (((4 + g4) ^ (r16 >> 1)) << 4);
      const unsigned qo = ST_QK + (8 * g4 + q4) * 64 + g1 * 32 + p4 * 8, ko = qo ^ 32;
      const unsigned vo0 = ST_V + (8 * g4 + 4 * g1 + q4) * 32 + p4 * 8, vo1 = vo0 ^ 128;
      f32x4 aq[4], ak[4], av[4];
#pragma unroll
      for (int m = 0; m < 4; ++m) { aq[m] = f32x4{}; ak[m] = f32x4{}; av[m] = f32x4{}; PIN(aq[m]); PIN(ak[m]); PIN(av[m]); }
      bf16x8 fx[2][4], fq[2], fk[2], fv[2];
#define LOAD_A(b, SB) { const lds_cptr B_ = (lds_cptr)L + (SB);                                                \
        fq[b] = ldtr2(B_ + qo + (b) * 2048, B_ + qo + (b) * 2048 + 256);                                       \
        _Pragma("unroll") for (int m = 0; m < 4; ++m) fx[b][m] = ld128(B_ + ((b) ? xo1 : xo0) + m * 2048);     \
        fk[b] = ldtr2(B_ + ko + (b) * 2048, B_ + ko + (b) * 2048 + 256);                                       \
        fv[b] = ldtr2(B_ + vo0 + (b) * 1024, B_ + vo1 + (b) * 1024); }
#define MMA_A(b) { _Pragma("unroll") for (int m = 0; m < 4; ++m) aq[m] = MFMA16(fq[b], fx[b][m], aq[m]);       \
        _Pragma("unroll") for (int m = 0; m < 4; ++m) ak[m] = MFMA16(fk[b], fx[b][m], ak[m]);                  \
        _Pragma("unroll") for (int m = 0; m < 4; ++m) av[m] = MFMA16(fx[b][m], fv[b], av[m]); }
      asm volatile("s_waitcnt vmcnt(0) lgkmcnt(0)\n\ts_barrier" ::: "memory");
      STAMP(10);
      int sb = ST_BYTES;
      LOAD_A(0, ST_BYTES)
      tile = qb * 8 + 2 * wid;
      const unsigned kvo = WS_KV + (h * 64 + tile) * 2048 + lane * 16;
#pragma unroll 1
      for (int c = 0; c < 8; ++c) {
        SBAR();
        LOAD_A(1, sb) MMA_A(0) INTERLEAVE(10)
        SBAR();
        sb = sb == 2 * ST_BYTES ? 0 : sb + ST_BYTES;
        if (c < 7) BAR();
        SBAR();
        LOAD_A(0, sb) MMA_A(1) INTERLEAVE(10)
        SBAR();
      }
      lds_arrive(lds0 + L_CNT + C_DONE * 4, 1, lane);
      STAMP(1);
      __builtin_amdgcn_s_setprio(3);
      const float qs = 0.25f * 1.4426950408889634f;
#pragma unroll
      for (int mt = 0; mt < 2; ++mt) {
        const u32x4 kv = merge16(ak[2 * mt], ak[2 * mt + 1], 1.0f), vv = merge32(av[2 * mt], av[2 * mt + 1]);
        st_sc1(kv, rs, kvo + mt * 2048); st_sc1(vv, rs, kvo + mt * 2048 + 1024);
        *(LAS u32x4*)(L + (2 * wid + mt) * 2048 + lane * 16) = kv;
        *(LAS u32x4*)(L + (2 * wid + mt) * 2048 + 1024 + lane * 16) = vv;
      }
      STAMP(5);
      *(LAS u32x4*)(L + L_QX + wid * 1024 + lane * 16) = merge16(aq[2], aq[3], qs);
      lds_arrive(lds0 + L_CNT + C_READY * 4, 2, lane);
      STAMP(6);
      qf = __builtin_bit_cast(bf16x8, merge16(aq[0], aq[1], qs));
    }
    const lds_cptr kp = (lds_cptr)L + lane * 16;
    const unsigned one = (r32 == 16 || r32 == 20) ? 0x3f803f80u : 0u;
    f32x16 negm, o = f32x16{}, sa;
    bf16x8 v0 = __builtin_bit_cast(bf16x8, u32x4{one, one, one, one}), v1 = v0, p0 = bf16x8{}, p1 = bf16x8{};
    unsigned ka = lds0 + lane * 16, va_ = lds0 + 1024 + hh * 256 + r16 * 16, ca = lds0 + L_CNT + C_READY * 4;
    asm volatile("" : "+{v[112:127]}"(o), "+{v[136:139]}"(v0), "+{v[140:143]}"(v1), "+{v[144:147]}"(p0), "+{v[148:151]}"(p1),
                 "+{v152}"(ka), "+{v153}"(va_), "+{v154}"(ca));
    if (wid < 4) flag_set(ws, WS_FKV + ((h * 4 + wid) * 8 + qb) * 8, make_tag(*(const LAS u64*)(L + L_E)), lane);
    STAMP(7);
    lds_wait(cnt + C_READY * 4, 8);
    STAMP(8);
    if (wid >= 4) qf = ld128((lds_cptr)L + L_QX + j * 1024 + lane * 16);
    bf16x8 kf = ld128(kp + 2048);
    {
      const bf16x8 k0 = ld128(kp);
      const f32x16 s0 = MFMA(k0, qf, f32x16{});
      const float m = swap_max(max16(s0));
#pragma unroll
      for (int r = 0; r < 16; ++r) negm[r] = -m;
      sa = MFMA(k0, qf, negm);
    }
    STAMP(2);
    float g = 0.f;
#pragma unroll 1
    for (int seg = 0; seg < 4; ++seg) {
      unsigned n_ = seg == 1 ? 4 : (seg == 2 ? 2 : 1), t_, sp_;
      if (seg < 2) __builtin_amdgcn_s_setprio(2); else if (seg == 2 || (PTW && wid >= 4)) __builtin_amdgcn_s_setprio(1); else __builtin_amdgcn_s_setprio(0);
      asm volatile(ATT_ASM
        : "+{v[64:79]}"(sa), "+{v[112:127]}"(o), "+{v[132:135]}"(kf), "+{v[136:139]}"(v0), "+{v[140:143]}"(v1), "+{v[144:147]}"(p0),
          "+{v[148:151]}"(p1), "+{v152}"(ka), "+{v153}"(va_), "+{v154}"(ca), [n] "+s"(n_), [tmp] "=&s"(t_), [spin] "=&s"(sp_)
        : "{v[96:111]}"(negm), "{v[128:131]}"(qf), [msk] "s"(0x0000ffff0000ffffull)
        : "v80", "v81", "v82", "v83", "v84", "v85", "v86", "v87", "v88", "v89", "v90", "v91", "v92", "v93", "v94", "v95",
          "v155", "scc", "memory");
      if (seg == 0) {
        STAMP(13);
        lds_wait(cnt + (C_READY + 1) * 4, 8);
        STAMP(14);
        sa = MFMA(ld128(kp + 16384), qf, negm);
        kf = ld128(kp + 16384 + 2048);
        ca = lds0 + L_CNT + (C_READY + 2) * 4;
      } else if (seg == 1) {
        lds_arrive(lds0 + L_CNT + C_HALF * 4, 1, lane);
      } else if (seg == 2) {
        lds_arrive(lds0 + L_CNT + C_LATE * 4, 1, lane);
        g = *(const LAS float*)(L + L_G + ((tile & 7) * 32 + r32) * 4);
      }
    }
    lds_arrive(lds0 + L_CNT + C_LATE * 4, 1, lane);
    __builtin_amdgcn_s_setprio(3);
    o = MFMA(v0, p0, o);
    o = MFMA(v1, p1, o);
    float chk = o[8] * 134217728.0f;
#pragma unroll
    for (int r = 0; r < 8; ++r) chk += fabsf(o[r]);
    const bool ok = chk < 1.7e38f;
    if (__builtin_expect(!__all(ok), 0)) {
      flags_wait(ws, WS_FKV + (h * 32 + r32) * 8, make_tag(*(const LAS u64*)(L + L_E)));
      float m = -INFINITY;
      o = f32x16{};
#pragma unroll 1
      for (int t = 0; t < 64; ++t) {
        const unsigned tb = WS_KV + (h * 64 + t) * 2048;
        const u32x4 kk = ld_sc1(rs, tb + lane * 16, 0);
        u32x4 va = ld_sc1(rs, tb + 1024 + hh * 256 + r16 * 16, 0), vb = ld_sc1(rs, tb + 1536 + hh * 256 + r16 * 16, 0);
        if (g1) { va = u32x4{one, one, one, one}; vb = va; }
        f32x16 z = MFMA(__builtin_bit_cast(bf16x8, kk), qf, f32x16{});
        const float mn = fmaxf(m, swap_max(max16(z)));
        const float al = __builtin_amdgcn_exp2f(m - mn);
        m = mn;
#pragma unroll
        for (int r = 0; r < 16; ++r) { z[r] = __builtin_amdgcn_exp2f(z[r] - m); o[r] *= al; }
        bf16x8 pa, pb; pack_p(z, pa, pb);
        o = MFMA(__builtin_bit_cast(bf16x8, va), pa, o);
        o = MFMA(__builtin_bit_cast(bf16x8, vb), pb, o);
      }
    }
    const float sc = g * __builtin_amdgcn_rcpf(o[8]);
    u32x4 ov;
#pragma unroll
    for (int i = 0; i < 4; ++i) ov[i] = cvtpk(o[2 * i] * sc, o[2 * i + 1] * sc);
    st_sc1(not_empty(ov), rs, WS_A + (tile * KSTEPS + h) * 1024 + lane * 16);
    STAMP(3);
    STAMP_OUT;
  } else {
    const int v = wid - 8;
    LANE_VARS DMA_VARS
    DMA_W1(0, lds0 + ST_BYTES) DMA_X(0, lds0 + ST_BYTES, 4)
    f32x4 ah[4][4];
    {
#pragma unroll
      for (int i = 0; i < 4; ++i) {
        const u32x2 bw = *(const u32x2*)(b1 + (hf * 64 + i * 16 + 4 * g4) * 2);
        ah[i][0] = f32x4{__uint_as_float(bw[0] << 16), __uint_as_float(bw[0] & 0xffff0000u), __uint_as_float(bw[1] << 16), __uint_as_float(bw[1] & 0xffff0000u)};
        ah[i][1] = ah[i][0]; ah[i][2] = ah[i][0]; ah[i][3] = ah[i][0];
      }
      const unsigned xo0 = (v * 64 + r16) * 128 + ((g4 ^ (r16 >> 1)) << 4), xo1 = (v * 64 + r16) * 128 + (((4 + g4) ^ (r16 >> 1)) << 4);
      const unsigned wo0 = ST_W1 + (8 * g4 + q4) * 128 + (((q4 >> 1) | (2 * g1)) * 32) + p4 * 8;
      bf16x8 fx[2][4], fw[2][4];
#define LOAD_F(b, SB) { const lds_cptr B_ = (lds_cptr)L + (SB);                                                \
        _Pragma("unroll") for (int m = 0; m < 4; ++m) {                                                        \
          fw[b][m] = ldtr2(B_ + (wo0 ^ (m * 32)) + (b) * 4096, B_ + (wo0 ^ (m * 32)) + (b) * 4096 + 512);      \
          fx[b][m] = ld128(B_ + ((b) ? xo1 : xo0) + m * 2048); } }
#define MMA_F(b) { _Pragma("unroll") for (int i = 0; i < 4; ++i) _Pragma("unroll") for (int m = 0; m < 4; ++m) \
          ah[i][m] = MFMA16(fw[b][i], fx[b][m], ah[i][m]); }
#pragma unroll
      for (int i = 0; i < 4; ++i) { PIN(ah[i][0]); PIN(ah[i][1]); PIN(ah[i][2]); PIN(ah[i][3]); }
      asm volatile("s_waitcnt vmcnt(0) lgkmcnt(0)\n\ts_barrier" ::: "memory");
      if (ffn) {
        int sb = ST_BYTES;
      LOAD_F(0, ST_BYTES)
#pragma unroll 1
      for (int c = 0; c < 8; ++c) {
        SBAR();
        LOAD_F(1, sb) MMA_F(0) INTERLEAVE(12)
        SBAR();
        sb = sb == 2 * ST_BYTES ? 0 : sb + ST_BYTES;
        if (c < 7) BAR();
        SBAR();
        LOAD_F(0, sb) MMA_F(1) INTERLEAVE(12)
        SBAR();
      }
      lds_arrive(lds0 + L_CNT + C_DONE * 4, 1, lane);
        STAMP(1);
      } else {
        unsigned go = r16 == 0 ? L_GATE + 16 * g4 : L_ZERO;
        const unsigned gstep = r16 == 0 ? 64 : 0;
        int sb = ST_BYTES;
#pragma unroll
        for (int m = 0; m < 4; ++m) ah[0][m] = f32x4{};
#pragma unroll 1
        for (int c = 0; c < 8; ++c) {
#pragma unroll
          for (int b = 0; b < 2; ++b) {
            const bf16x8 fg = ld128((lds_cptr)L + go); go += gstep;
#pragma unroll
            for (int m = 0; m < 4; ++m) ah[0][m] = MFMA16(fg, ld128((lds_cptr)L + sb + (b ? xo1 : xo0) + m * 2048), ah[0][m]);
          }
          sb = sb == 2 * ST_BYTES ? 0 : sb + ST_BYTES;
          if (c < 7) BAR();
        }
        lds_arrive(lds0 + L_CNT + C_DONE * 4, 1, lane);
        if (h == 7 && lane < 16) {
#pragma unroll
          for (int m = 0; m < 4; ++m)
            __hip_atomic_store((GAS float*)(ws + WS_G) + qb * 256 + v * 64 + m * 16 + lane, gate_from_logit(ah[0][m][0]), RLX_AGENT);
        }
      }
    }
    __builtin_amdgcn_s_setprio(3);
    const u64 tag = make_tag(*(const LAS u64*)(L + L_E));
    STAMP(2);
    const lds_ptr kw = L + v * 4096 + lane * 16;
    const unsigned kvb = WS_KV + (h * 64 + 2 * v) * 2048, fkv = WS_FKV + (h * 4 + v) * 64;
    unsigned got = 0;
    {
      u32x4 hp[8];
#pragma unroll
      for (int mt = 0; mt < 2; ++mt)
#pragma unroll
        for (int i = 0; i < 4; ++i) {
          f32x4 a = ah[i][2 * mt], b = ah[i][2 * mt + 1];
#pragma unroll
          for (int r = 0; r < 4; ++r) { a[r] = relu(a[r]); b[r] = relu(b[r]); }
          a *= a; b *= b;
          hp[mt * 4 + i] = merge16(a, b, 1.0f); PIN(hp[mt * 4 + i]);
        }
      gu64* fl = (gu64*)(ws + fkv + SRC(lane & 7) * 8);
#pragma unroll 1
      for (unsigned spins = 0; spins < (SPIN_CAP >> 4); ++spins) {
        got = (unsigned)__ballot(__hip_atomic_load(fl, RLX_AGENT) == tag) & 0xfeu;
        if (W23 ? (got & 6) == 6 : (got & 2)) break;
        __builtin_amdgcn_s_sleep(1);
      }
      asm volatile("" ::: "memory");
      STAMP(8);
      {
        u32x4 kr[GA][4];
#pragma unroll
        for (int c = 0; c < GA; ++c) {
          const unsigned vo = lane * 16 + ((got >> (c + 1) & 1) ? kvb + SRC(1 + c) * 16384 : OOB);
#pragma unroll
          for (int i = 0; i < 4; ++i) kr[c][i] = ld_sc1(rs, vo + i * 1024, 0);
        }
        const u64 f2 = __hip_atomic_load(fl, RLX_AGENT);
        SBAR();
        lds_wait(cnt + C_DONE * 4, 8);
#pragma unroll
        for (int c = 0; c < GA; ++c)
          if (got >> (c + 1) & 1) {
#pragma unroll
            for (int i = 0; i < 4; ++i) *(LAS u32x4*)(kw + (1 + c) * 16384 + i * 1024) = kr[c][i];
            lds_arrive(lds0 + L_CNT + (C_READY + 1 + c) * 4, 2, lane);
          }
        STAMP(13);
        SBAR();
#pragma unroll
        for (int i = 0; i < 8; ++i)
          st_sc1(hp[i], rs, (ffn ? WS_A + ((qb * 8 + v * 2 + (i >> 2)) * KSTEPS + 32 + hf * 4 + (i & 3)) * 1024 : OOB) + lane * 16);
        SBAR();
        flag_set(ws, WS_FH + ((qb * 4 + v) * 32 + h) * 8, tag, lane);
        STAMP(10);
        if (!__all(f2 == tag)) flags_wait(ws, fkv + (lane & 7) * 8, tag);
        asm volatile("" ::: "memory");
      }
      STAMP(3);
    }
    STAMP(4);
    f32x16 acc[4];
    const int wx = (q4 >> 1) * 64;
    bool wo_done = false;
#define WO_DMA() { const char* wsrc_ = wo + (size_t)v * 128 * 1024 + ct * 128;                                   \
      const unsigned wvo_ = (lane >> 3) * 1024 + (((lane & 7) ^ (4 * g1)) << 4);                                 \
      _Pragma("unroll") for (int i = 0; i < 16; ++i) glds16(wsrc_ + i * 8192, wvo_, lds0 + v * 16384 + i * 1024); }
    {
      constexpr int NS = 28, PD = 4;
      const __amdgpu_buffer_rsrc_t rw = __builtin_amdgcn_make_buffer_rsrc((void*)w2, 0, FF * 1024, 0x00020000);
      const unsigned wvo = (lane >> 3) * 1024 + (((lane & 7) ^ (4 * g1)) << 4), wso = v * 448 * 1024 + ct * 128;
      const unsigned ao = WS_A + ((qb * 8 + u * 2 + (v & 1)) * KSTEPS + 32 + v * 28) * 1024, ao1 = (v & 1) ? ao - KSTEPS * 1024 : ao + KSTEPS * 1024;
      u32x4 wr[PD][2], ar[PD][2];
      const int rot2 = ROT2 ? (3 * ct + 7 * (qb & 3)) % NS : 0;
#define FETCHW(i_, slot) { const int i = (i_) + rot2 - ((i_) + rot2 >= NS ? NS : 0);                             \
        wr[slot][0] = __builtin_amdgcn_raw_buffer_load_b128(rw, wvo, wso + i * 16384, 0); wr[slot][1] = __builtin_amdgcn_raw_buffer_load_b128(rw, wvo, wso + i * 16384 + 8192, 0); }
#define FETCHA(i_, slot) { const int i = (i_) + rot2 - ((i_) + rot2 >= NS ? NS : 0);                             \
        ar[slot][0] = ld_sc1(rs, lane * 16, ao + i * 1024); ar[slot][1] = ld_sc1(rs, lane * 16, ao1 + i * 1024); }
#pragma unroll
      for (int k = 0; k < PD; ++k) FETCHW(k, k)
      const unsigned short bb[2] = {*(const unsigned short*)(b2 + (ct * 64 + r32) * 2), *(const unsigned short*)(b2 + (ct * 64 + 32 + r32) * 2)};
      const unsigned fho = WS_FH + ((lane & 7) == 7 ? (qb * 4 + v) * 32 + 7 : (qb * 4 + u) * 32 + 8 * v + (lane & 7)) * 8;
      bool fh_ok;
      {
        u32x4 kr[6][4];
#pragma unroll
        for (int c = 0; c < 6; ++c) {
          const unsigned vo = lane * 16 + ((c + 1 < GA && (got >> (c + 2) & 1)) ? OOB : kvb + SRC(2 + c) * 16384);
#pragma unroll
          for (int i = 0; i < 4; ++i) kr[c][i] = ld_sc1(rs, vo + i * 1024, 0);
        }
        const u64 fh = __hip_atomic_load((gu64*)(ws + fho), RLX_AGENT);
        const unsigned wt = *(const unsigned*)(wo + (size_t)(v * 128 + (qb & 1) * 64 + lane) * 1024 + ct * 128);
#pragma unroll
        for (int i = 0; i < 2; ++i) st_sc1(u32x4{~0u, ~0u, ~0u, ~0u}, rs, WS_A + ((qb * 8 + v * 2 + i) * KSTEPS + h) * 1024 + lane * 16);
#pragma unroll
        for (int c = 0; c < 6; ++c)
          if (!(c + 1 < GA && (got >> (c + 2) & 1))) {
#pragma unroll
            for (int i = 0; i < 4; ++i) *(LAS u32x4*)(kw + (2 + c) * 16384 + i * 1024) = kr[c][i];
            if (c < 5) lds_arrive(lds0 + L_CNT + (C_READY + 2 + c) * 4, 2, lane);
          }
        STAMP(11);
        flag_set(ws, WS_FO + ((qb * 4 + v) * 32 + h) * 8, tag, lane);
        asm volatile("" :: "v"(wt));
        fh_ok = __all(fh == tag);
      }
      if (!fh_ok) flags_wait(ws, fho, tag);
      asm volatile("" ::: "memory");
      STAMP(5);
      const float zg = __hip_atomic_load((GAS float*)(ws + WS_G) + qb * 256 + v * 64 + lane, RLX_AGENT);
#pragma unroll
      for (int k = 0; k < PD; ++k) FETCHA(k, k)
      {
        const bf16x8 e1 = __builtin_bit_cast(bf16x8, u32x4{hh ? 0u : 0x3f80u, 0u, 0u, 0u});
#pragma unroll
        for (int c2 = 0; c2 < 2; ++c2) {
          const bf16x8 bf = __builtin_bit_cast(bf16x8, u32x4{v == 0 ? (unsigned)bb[c2] : 0u, 0u, 0u, 0u});
          acc[2 * c2] = MFMA(bf, e1, f32x16{}); PIN(acc[2 * c2]);
          acc[2 * c2 + 1] = MFMA(bf, e1, f32x16{}); PIN(acc[2 * c2 + 1]);
        }
      }
      *(LAS float*)(L + L_G + (v * 64 + lane) * 4) = zg;
      lds_arrive(lds0 + L_CNT + (C_READY + 7) * 4, 2, lane);
      const lds_ptr wd = L + L_SCR + v * 4096 + lane * 16;
      unsigned ra_ = lds0 + L_SCR + v * 4096 + (8 * hh + q4) * 128 + g1 * 32 + p4 * 8 + (wx ^ ((v >> 1) * 64)), rb_ = ra_ ^ 64;
      PIN(ra_); PIN(rb_);
      const lds_cptr ra = (lds_cptr)(uintptr_t)ra_, rb = (lds_cptr)(uintptr_t)rb_;
#define PACE() { SBAR(); if (PSLEEP) __builtin_amdgcn_s_sleep(PSLEEP); SBAR(); }
#define STAGE2(j, wa, wb) { const int buf = ((j) & 1) * 2048;                                                  \
        *(LAS u32x4*)(wd + buf) = wr[j][0]; *(LAS u32x4*)(wd + buf + 1024) = wr[j][1];                          \
        wa = ldtr2(ra + buf, ra + buf + 512);                                                     \
        wb = ldtr2(rb + buf, rb + buf + 512); }
      bf16x8 wa[2], wb[2];
      STAGE2(0, wa[0], wb[0])
#pragma unroll 1
      for (int i0 = 0; i0 < (NO_FFN2 ? 4 : NS); i0 += PD) {
        if (i0 == HISTEPS) __builtin_amdgcn_s_setprio(APRIO);
        if (!wo_done && i0 >= 12 && __builtin_amdgcn_readfirstlane(*(const volatile LAS unsigned*)(cnt + C_HALF * 4)) >= 8) { WO_DMA() wo_done = true; }
#pragma unroll
        for (int j = 0; j < PD; ++j) {
          const int in = i0 + PD + j < NS ? i0 + PD + j : NS - 1;
          SBAR();
          if (ABL & 1) { PIN(wr[j][0]); PIN(wr[j][1]); } else FETCHW(in, j)
          STAGE2((j + 1) % PD, wa[(j + 1) & 1], wb[(j + 1) & 1])
          SBAR();
          const bf16x8 a0 = __builtin_bit_cast(bf16x8, ar[j][0]), a1 = __builtin_bit_cast(bf16x8, ar[j][1]);
          acc[0] = MFMA(wa[j & 1], a0, acc[0]); PACE()
          acc[1] = MFMA(wa[j & 1], a1, acc[1]); PACE()
          acc[2] = MFMA(wb[j & 1], a0, acc[2]); PACE()
          acc[3] = MFMA(wb[j & 1], a1, acc[3]);
          SBAR();
          if (ABL & 2) { PIN(ar[j][0]); PIN(ar[j][1]); } else FETCHA(in, j)
        }
      }
    }
    STAMP(6);
    __builtin_amdgcn_s_setprio(3);
    {
    LANE_VARS
    const int wx = (q4 >> 1) * 64;
    if (!wo_done) { lds_wait_slow(cnt + C_HALF * 4, 8); WO_DMA() }
    const int mt = v & 1, kh = v >> 1;
    f32x16 fo[2];
    {
#define XSEND(S0, S1, K0, K1) { _Pragma("unroll") for (int rq = 0; rq < 4; ++rq) {                               \
        *(LAS f32x4*)(L + XCH(v, 0) + rq * 1024 + lane * 16) = f32x4{S0[4 * rq], S0[4 * rq + 1], S0[4 * rq + 2], S0[4 * rq + 3]}; \
        *(LAS f32x4*)(L + XCH(v, 1) + rq * 1024 + lane * 16) = f32x4{S1[4 * rq], S1[4 * rq + 1], S1[4 * rq + 2], S1[4 * rq + 3]}; } \
        fo[0] = K0; fo[1] = K1; }
      XSEND(acc[1], acc[3], acc[0], acc[2])
      flags_wait(ws, WS_FO + ((qb * 4 + u) * 32 + kh * 16 + r16) * 8, tag);
      lds_arrive(lds0 + L_CNT + (C_X0 + kh) * 4, 1, lane);
      lds_wait(cnt + (C_READY + 7) * 4, 8);
      const float og = bfround(1.0f - *(const LAS float*)(L + L_G + (u * 64 + mt * 32 + r32) * 4));
      lds_wait(cnt + (C_X0 + kh) * 4, 2);
#pragma unroll
      for (int c2 = 0; c2 < 2; ++c2) {
#pragma unroll
        for (int rq = 0; rq < 4; ++rq) {
          const f32x4 t = *(const LAS f32x4*)(L + XCH(v ^ 1, c2) + rq * 1024 + lane * 16);
#pragma unroll
          for (int r = 0; r < 4; ++r) fo[c2][4 * rq + r] = (fo[c2][4 * rq + r] + t[r]) * og;
        }
        PIN(fo[c2]);
      }
    }
    STAMP(7);
    {
      LANE_VARS
      const int wx = (q4 >> 1) * 64;
      const lds_cptr rsrc = (lds_cptr)L + kh * 32768 + (4 * hh + q4) * 128 + g1 * 32 + p4 * 8;
      const char* ob = ws + WS_A + (size_t)((qb * 8 + u * 2 + mt) * KSTEPS + kh * 16) * 1024;
      lds_wait_slow(cnt + C_LATE * 4, LATEN);
      unsigned todo, nx, sp, m, t, thr;
      asm volatile(TAIL_ASM
        : "+{v[32:47]}"(fo[0]), "+{v[48:63]}"(fo[1]), [todo] "=&s"(todo), [nx] "=&s"(nx), [sp] "=&s"(sp), [m] "=&s"(m), [t] "=&s"(t), [thr] "=&s"(thr)
        : [la] "v"((unsigned)(uintptr_t)(rsrc + (wx ^ (kh * 64)))), [lb] "v"((unsigned)(uintptr_t)(rsrc + (wx ^ (kh * 64) ^ 64))), [vo] "v"(lane * 16),
          [vp] "v"(r16 * 1024 + 1020), [ce] "v"(lds0 + L_CNT + C_LATE * 4), [ob] "s"(ob)
        : TAIL_CLOB, "vcc", "scc", "memory");
    }
    STAMP(9);
    {
    LANE_VARS
    f32x16 fin = fo[0];
    {
      const f32x16 snd = fo[1];
#pragma unroll
      for (int rq = 0; rq < 4; ++rq)
        *(LAS f32x4*)(L + XCH(v ^ 1, 0) + rq * 1024 + lane * 16) = f32x4{snd[4 * rq], snd[4 * rq + 1], snd[4 * rq + 2], snd[4 * rq + 3]};
      lds_arrive(lds0 + L_CNT + (C_FIN + mt) * 4, 1, lane);
      lds_wait(cnt + (C_FIN + mt) * 4, 2);
#pragma unroll
      for (int rq = 0; rq < 4; ++rq) {
        const f32x4 t = *(const LAS f32x4*)(L + XCH(v ^ 3, 0) + rq * 1024 + lane * 16);
        fin[4 * rq] += t[0]; fin[4 * rq + 1] += t[1]; fin[4 * rq + 2] += t[2]; fin[4 * rq + 3] += t[3];
      }
    }
    char* op = out + (size_t)(qb * 256 + u * 64 + mt * 32 + r32) * 1024 + (ct * 64 + kh * 32 + 8 * hh) * 2;
#pragma unroll
    for (int pr = 0; pr < 2; ++pr)
      *(u32x4*)(op + pr * 32) = merge32(f32x4{fin[8 * pr], fin[8 * pr + 1], fin[8 * pr + 2], fin[8 * pr + 3]},
                                        f32x4{fin[8 * pr + 4], fin[8 * pr + 5], fin[8 * pr + 6], fin[8 * pr + 7]});
    if (blockIdx.x == 0 && v == 0 && lane == 0) __hip_atomic_store((gu64*)(ws + WS_E), tag, RLX_AGENT);
    }
    }
    STAMP(12);
    STAMP_OUT;
  }
}

extern "C" void kernel_launch(void* const* d_in, const int* in_sizes, int n_in, void* d_out, int out_size,
                              void* d_ws, size_t ws_size, hipStream_t stream) {
  (void)hipFuncSetAttribute((const void*)fused_k, hipFuncAttributeMaxDynamicSharedMemorySize, LDS_BYTES);
  fused_k<<<256, 768, LDS_BYTES, stream>>>((const char*)d_in[0], (const char*)d_in[1], (const char*)d_in[2],
                                           (const char*)d_in[4], (const char*)d_in[5], (const char*)d_in[6],
                                           (const char*)d_in[7], (const char*)d_in[8], (char*)d_ws, (char*)d_out);
}
```

```cpp
#include <hip/hip_runtime.h>
#include <hip/hip_bf16.h>
#include <stdint.h>

typedef __attribute__((ext_vector_type(8))) __bf16 bf16x8;
typedef __attribute__((ext_vector_type(16))) __bf16 bf16x16;
typedef __attribute__((ext_vector_type(8))) float f32x8;
typedef __attribute__((ext_vector_type(2))) __bf16 bf16x2;
typedef __attribute__((ext_vector_type(4))) short s16x4;
typedef __attribute__((ext_vector_type(8))) short s16x8;
typedef __attribute__((ext_vector_type(16))) float f32x16;
typedef __attribute__((ext_vector_type(4))) float f32x4;
typedef __attribute__((ext_vector_type(4))) unsigned u32x4;
typedef __attribute__((ext_vector_type(2))) unsigned u32x2;
typedef unsigned long long u64;
#define LAS __attribute__((address_space(3)))
#define GAS __attribute__((address_space(1)))
typedef LAS const char* lds_cptr;
typedef LAS char* lds_ptr;
typedef GAS u64 gu64;

constexpr int FF = 1792;
constexpr int KSTEPS = 32 + FF / 16;

constexpr unsigned WS_E = 0;
constexpr unsigned WS_FKV = 4096;
constexpr unsigned WS_FH = WS_FKV + 1024 * 128;
constexpr unsigned WS_FO = WS_FH + 1024 * 128;
constexpr unsigned WS_G = WS_FO + 1024 * 128;
constexpr unsigned WS_KV = 1u << 20;
constexpr unsigned WS_A = WS_KV + 32u * 64 * 2048;
constexpr unsigned WS_END = WS_A + 64u * KSTEPS * 1024;

constexpr unsigned OOB = 0x40000000u;

constexpr int ST_QK = 32768, ST_V = ST_QK + 4096, ST_W1 = ST_V + 2048, ST_BYTES = ST_W1 + 8192;
constexpr int L_SCR = 131072;
constexpr int L_GATE = L_SCR + 24576, L_QX = L_GATE + 1024, L_G = L_QX + 4096, L_E = L_G + 1024, L_CNT = L_E + 16;
constexpr int C_DONE = 0, C_HALF = 1, C_END = 2, C_WO = 3, C_FIN = 4, C_PRE = 8, C_READY = 16;
constexpr int L_ZERO = L_CNT + 28 * 4;
constexpr int LDS_BYTES = L_CNT + 128;

#define MFMA(a, b, c) __builtin_amdgcn_mfma_f32_32x32x16_bf16(a, b, c, 0, 0, 0)
#define MFMA16(a, b, c) __builtin_amdgcn_mfma_f32_16x16x32_bf16(a, b, c, 0, 0, 0)
#define INTERLEAVE(n) { _Pragma("unroll") for (int i_ = 0; i_ < (n); ++i_) { __builtin_amdgcn_sched_group_barrier(0x008, 1, 0); __builtin_amdgcn_sched_group_barrier(0x100, 1, 0); } __builtin_amdgcn_sched_group_barrier(0x008, 16, 0); }
#define BAR() asm volatile("s_waitcnt lgkmcnt(0)\n\ts_barrier" ::: "memory")
#define LANE_VARS int lane; asm volatile("v_mbcnt_lo_u32_b32 %0, -1, 0\n\tv_mbcnt_hi_u32_b32 %0, -1, %0" : "=v"(lane)); __builtin_assume(lane >= 0 && lane < 64);                       \
  const int r32 = lane & 31, hh = lane >> 5, g1 = (lane >> 4) & 1, g4 = lane >> 4, r16 = lane & 15, q4 = (lane >> 2) & 3, p4 = lane & 3; \
  (void)r32; (void)hh; (void)g1; (void)g4; (void)r16; (void)q4; (void)p4;
#define DMA_VARS                                                                                                 \
  const unsigned xv0 = (lane >> 3) * 1024 + (((lane & 7) ^ g4) << 4), xv1 = xv0 ^ 64;                            \
  const unsigned qv_ = (lane >> 2) * 3072 + (((p4 >> 1) ^ hh) << 10) + (p4 & 1) * 16;                            \
  const unsigned vv_ = ((r32 >> 1) ^ (4 * g1)) * 3072 + 2048 + (lane & 1) * 16;                                  \
  const unsigned wv0 = (lane >> 3) * (FF * 2) + (((lane & 7) ^ (2 * g1)) << 4), wv1 = wv0 ^ 64;                  \
  (void)qv_; (void)vv_; (void)wv0; (void)wv1;
#define SBAR() __builtin_amdgcn_sched_barrier(0)
#define PIN(x) asm volatile("" : "+v"(x))
#define RLX_AGENT __ATOMIC_RELAXED, __HIP_MEMORY_SCOPE_AGENT
#define SC1 16

__device__ __forceinline__ unsigned cvtpk(float lo, float hi) {
  bf16x2 v; v[0] = (__bf16)lo; v[1] = (__bf16)hi; return __builtin_bit_cast(unsigned, v);
}
__device__ __forceinline__ float bfround(float x) { return (float)(__bf16)x; }
__device__ __forceinline__ bf16x8 ldtr2(lds_cptr p0, lds_cptr p1) {
  s16x4 a = __builtin_amdgcn_ds_read_tr16_b64_v4i16((LAS s16x4*)p0), b = __builtin_amdgcn_ds_read_tr16_b64_v4i16((LAS s16x4*)p1);
  s16x8 r = {a[0], a[1], a[2], a[3], b[0], b[1], b[2], b[3]};
  return __builtin_bit_cast(bf16x8, r);
}
__device__ __forceinline__ bf16x8 ld128(lds_cptr p) { return *(const LAS bf16x8*)p; }
__device__ __forceinline__ void glds16(const char* sbase, unsigned voff, unsigned lds_base) {
  unsigned sv;
  asm volatile("s_mov_b32 %0, m0\n\ts_mov_b32 m0, %3\n\ts_nop 0\n\tglobal_load_lds_dwordx4 %1, %2\n\ts_mov_b32 m0, %0"
               : "=&s"(sv) : "v"(voff), "s"(sbase), "s"(__builtin_amdgcn_readfirstlane(lds_base)) : "memory");
}
__device__ __forceinline__ void glds16_sc1(const char* sbase, unsigned voff, unsigned lds_base) {
  unsigned sv;
  asm volatile("s_mov_b32 %0, m0\n\ts_mov_b32 m0, %3\n\ts_nop 0\n\tglobal_load_lds_dwordx4 %1, %2 sc1\n\ts_mov_b32 m0, %0"
               : "=&s"(sv) : "v"(voff), "s"(sbase), "s"(__builtin_amdgcn_readfirstlane(lds_base)) : "memory");
}
__device__ __forceinline__ void st_sc1(u32x4 v, __amdgpu_buffer_rsrc_t rs, unsigned off) { __builtin_amdgcn_raw_buffer_store_b128(v, rs, off, 0, SC1); }
__device__ __forceinline__ float swap_max(float v) {
  auto rr = __builtin_amdgcn_permlane32_swap(__float_as_uint(v), __float_as_uint(v), false, false);
  return fmaxf(__uint_as_float(rr[0]), __uint_as_float(rr[1]));
}
__device__ __forceinline__ u32x4 merge16(f32x4 a, f32x4 b, float s) {
  auto r0 = __builtin_amdgcn_permlane16_swap(cvtpk(a[0] * s, a[1] * s), cvtpk(b[0] * s, b[1] * s), false, false);
  auto r1 = __builtin_amdgcn_permlane16_swap(cvtpk(a[2] * s, a[3] * s), cvtpk(b[2] * s, b[3] * s), false, false);
  return u32x4{r0[0], r1[0], r0[1], r1[1]};
}
__device__ __forceinline__ u32x4 not_empty(u32x4 v) { return u32x4{min(v[0], 0xfffffffeu), min(v[1], 0xfffffffeu), min(v[2], 0xfffffffeu), min(v[3], 0xfffffffeu)}; }
__device__ __forceinline__ u32x4 ld_sc1(__amdgpu_buffer_rsrc_t rs, unsigned voff, unsigned soff) { return __builtin_amdgcn_raw_buffer_load_b128(rs, voff, soff, SC1); }
__device__ __forceinline__ float relu(float x) { return __builtin_amdgcn_fmed3f(x, 0.f, 3.4028235e38f); }
__device__ __forceinline__ float gate_from_logit(float z) {
  z = bfround(z);
  return bfround(1.0f / (1.0f + __expf(-z)));
}
__device__ __forceinline__ float max16(const f32x16& s) {
  float a = fmaxf(fmaxf(s[0], s[1]), s[2]);
#pragma unroll
  for (int r = 3; r < 15; r += 2) a = fmaxf(fmaxf(a, s[r]), s[r + 1]);
  return fmaxf(a, s[15]);
}
__device__ __forceinline__ f32x16 pv(bf16x8 v, const f32x16& p, int half, f32x16 o) {
  const f32x8 b = {p[8 * half], p[8 * half + 1], p[8 * half + 2], p[8 * half + 3], p[8 * half + 4], p[8 * half + 5], p[8 * half + 6], p[8 * half + 7]};
  return __builtin_amdgcn_smfmac_f32_32x32x32_bf16(v, __builtin_bit_cast(bf16x16, b), o, 0xdddd, 0, 0);
}
__device__ __forceinline__ u64 make_tag(u64 e) {
  const unsigned lo = __builtin_amdgcn_readfirstlane((unsigned)e), hi = __builtin_amdgcn_readfirstlane((unsigned)(e >> 32));
  return (((u64)hi << 32) | lo) * 0x9E3779B97F4A7C15ull + 0xD1B54A32D192ED03ull;
}
__device__ __forceinline__ void lds_arrive(unsigned addr, unsigned n, int lane) {
  asm volatile("s_waitcnt lgkmcnt(0)" ::: "memory");
  if (lane == 0) asm volatile("ds_add_u32 %0, %1" :: "v"(addr), "v"(n) : "memory");
}
constexpr unsigned SPIN_CAP = 1u << 18;
__device__ __forceinline__ void lds_wait(lds_cptr p, unsigned want) {
#pragma unroll 1
  for (unsigned spins = 0; spins < SPIN_CAP; ++spins) {
    const unsigned v = __builtin_amdgcn_readfirstlane(*(const volatile LAS unsigned*)p);
    if (v >= want) break;
    __builtin_amdgcn_s_sleep(1);
  }
  asm volatile("" ::: "memory");
}
__device__ __forceinline__ void flags_wait(const char* ws, unsigned off, u64 tag) {
  gu64* f = (gu64*)(ws + off);
#pragma unroll 1
  for (unsigned spins = 0; spins < (SPIN_CAP >> 4); ++spins) {
    const bool ok = __hip_atomic_load(f, RLX_AGENT) == tag;
    if (__all(ok)) break;
    __builtin_amdgcn_s_sleep(1);
  }
  asm volatile("s_waitcnt vmcnt(0)" ::: "memory");
}
__device__ __forceinline__ void flag_set(char* ws, unsigned off, u64 tag, int lane) {
  asm volatile("s_waitcnt vmcnt(0)" ::: "memory");
  if (lane == 0) __hip_atomic_store((gu64*)(ws + off), tag, RLX_AGENT);
}

#define ATT_ASM \
  "s_nop 15\n" \
  "Latt_loop%=:\n" \
  "v_exp_f32 v64, v64\n" \
  "v_exp_f32 v65, v65\n" \
  "v_smfmac_f32_32x32x32_bf16 v[112:127], v[140:143], v[88:95], v156\n" \
  "s_mov_b64 exec, %[msk]\n" \
  "ds_read_b128 v[136:139], v153 offset:2048\n" \
  "ds_read_b128 v[140:143], v153 offset:2304\n" \
  "s_mov_b64 exec, -1\n" \
  "v_exp_f32 v66, v66\n" \
  "v_exp_f32 v67, v67\n" \
  "v_exp_f32 v68, v68\n" \
  "v_exp_f32 v69, v69\n" \
  "s_waitcnt lgkmcnt(2)\n" \
  "v_mfma_f32_32x32x16_bf16 v[80:95], v[132:135], v[128:131], v[96:111]\n" \
  "ds_read_b128 v[132:135], v152 offset:4096\n" \
  "v_exp_f32 v70, v70\n" \
  "v_exp_f32 v71, v71\n" \
  "v_exp_f32 v72, v72\n" \
  "v_exp_f32 v73, v73\n" \
  "v_exp_f32 v74, v74\n" \
  "v_smfmac_f32_32x32x32_bf16 v[112:127], v[144:147], v[64:71], v156\n" \
  "v_exp_f32 v75, v75\n" \
  "v_exp_f32 v76, v76\n" \
  "v_exp_f32 v77, v77\n" \
  "v_exp_f32 v78, v78\n" \
  "v_exp_f32 v79, v79\n" \
  "v_exp_f32 v80, v80\n" \
  "v_exp_f32 v81, v81\n" \
  "v_smfmac_f32_32x32x32_bf16 v[112:127], v[148:151], v[72:79], v156\n" \
  "s_mov_b64 exec, %[msk]\n" \
  "ds_read_b128 v[144:147], v153 offset:4096\n" \
  "ds_read_b128 v[148:151], v153 offset:4352\n" \
  "s_mov_b64 exec, -1\n" \
  "v_exp_f32 v82, v82\n" \
  "v_exp_f32 v83, v83\n" \
  "v_exp_f32 v84, v84\n" \
  "v_exp_f32 v85, v85\n" \
  "s_waitcnt lgkmcnt(2)\n" \
  "v_mfma_f32_32x32x16_bf16 v[64:79], v[132:135], v[128:131], v[96:111]\n" \
  "ds_read_b128 v[132:135], v152 offset:6144\n" \
  "v_exp_f32 v86, v86\n" \
  "v_exp_f32 v87, v87\n" \
  "v_exp_f32 v88, v88\n" \
  "v_exp_f32 v89, v89\n" \
  "v_exp_f32 v90, v90\n" \
  "v_smfmac_f32_32x32x32_bf16 v[112:127], v[136:139], v[80:87], v156\n" \
  "v_exp_f32 v91, v91\n" \
  "v_exp_f32 v92, v92\n" \
  "v_exp_f32 v93, v93\n" \
  "v_exp_f32 v94, v94\n" \
  "v_exp_f32 v95, v95\n" \
  "v_exp_f32 v64, v64\n" \
  "v_exp_f32 v65, v65\n" \
  "v_smfmac_f32_32x32x32_bf16 v[112:127], v[140:143], v[88:95], v156\n" \
  "s_mov_b64 exec, %[msk]\n" \
  "ds_read_b128 v[136:139], v153 offset:6144\n" \
  "ds_read_b128 v[140:143], v153 offset:6400\n" \
  "s_mov_b64 exec, -1\n" \
  "v_exp_f32 v66, v66\n" \
  "v_exp_f32 v67, v67\n" \
  "v_exp_f32 v68, v68\n" \
  "v_exp_f32 v69, v69\n" \
  "s_waitcnt lgkmcnt(2)\n" \
  "v_mfma_f32_32x32x16_bf16 v[80:95], v[132:135], v[128:131], v[96:111]\n" \
  "ds_read_b128 v[132:135], v152 offset:8192\n" \
  "v_exp_f32 v70, v70\n" \
  "v_exp_f32 v71, v71\n" \
  "v_exp_f32 v72, v72\n" \
  "v_exp_f32 v73, v73\n" \
  "v_exp_f32 v74, v74\n" \
  "v_smfmac_f32_32x32x32_bf16 v[112:127], v[144:147], v[64:71], v156\n" \
  "v_exp_f32 v75, v75\n" \
  "v_exp_f32 v76, v76\n" \
  "v_exp_f32 v77, v77\n" \
  "v_exp_f32 v78, v78\n" \
  "v_exp_f32 v79, v79\n" \
  "v_exp_f32 v80, v80\n" \
  "v_exp_f32 v81, v81\n" \
  "v_smfmac_f32_32x32x32_bf16 v[112:127], v[148:151], v[72:79], v156\n" \
  "s_mov_b64 exec, %[msk]\n" \
  "ds_read_b128 v[144:147], v153 offset:8192\n" \
  "ds_read_b128 v[148:151], v153 offset:8448\n" \
  "s_mov_b64 exec, -1\n" \
  "v_exp_f32 v82, v82\n" \
  "v_exp_f32 v83, v83\n" \
  "v_exp_f32 v84, v84\n" \
  "v_exp_f32 v85, v85\n" \
  "s_waitcnt lgkmcnt(2)\n" \
  "v_mfma_f32_32x32x16_bf16 v[64:79], v[132:135], v[128:131], v[96:111]\n" \
  "ds_read_b128 v[132:135], v152 offset:10240\n" \
  "v_exp_f32 v86, v86\n" \
  "v_exp_f32 v87, v87\n" \
  "v_exp_f32 v88, v88\n" \
  "v_exp_f32 v89, v89\n" \
  "v_exp_f32 v90, v90\n" \
  "v_smfmac_f32_32x32x32_bf16 v[112:127], v[136:139], v[80:87], v156\n" \
  "v_exp_f32 v91, v91\n" \
  "v_exp_f32 v92, v92\n" \
  "v_exp_f32 v93, v93\n" \
  "v_exp_f32 v94, v94\n" \
  "v_exp_f32 v95, v95\n" \
  "v_exp_f32 v64, v64\n" \
  "v_exp_f32 v65, v65\n" \
  "v_smfmac_f32_32x32x32_bf16 v[112:127], v[140:143], v[88:95], v156\n" \
  "s_mov_b64 exec, %[msk]\n" \
  "ds_read_b128 v[136:139], v153 offset:10240\n" \
  "ds_read_b128 v[140:143], v153 offset:10496\n" \
  "s_mov_b64 exec, -1\n" \
  "v_exp_f32 v66, v66\n" \
  "v_exp_f32 v67, v67\n" \
  "v_exp_f32 v68, v68\n" \
  "v_exp_f32 v69, v69\n" \
  "s_waitcnt lgkmcnt(2)\n" \
  "v_mfma_f32_32x32x16_bf16 v[80:95], v[132:135], v[128:131], v[96:111]\n" \
  "ds_read_b128 v[132:135], v152 offset:12288\n" \
  "ds_read_b32 v155, v154\n" \
  "v_exp_f32 v70, v70\n" \
  "v_exp_f32 v71, v71\n" \
  "v_exp_f32 v72, v72\n" \
  "v_exp_f32 v73, v73\n" \
  "v_exp_f32 v74, v74\n" \
  "v_smfmac_f32_32x32x32_bf16 v[112:127], v[144:147], v[64:71], v156\n" \
  "v_exp_f32 v75, v75\n" \
  "v_exp_f32 v76, v76\n" \
  "v_exp_f32 v77, v77\n" \
  "v_exp_f32 v78, v78\n" \
  "v_exp_f32 v79, v79\n" \
  "v_exp_f32 v80, v80\n" \
  "v_exp_f32 v81, v81\n" \
  "v_smfmac_f32_32x32x32_bf16 v[112:127], v[148:151], v[72:79], v156\n" \
  "s_mov_b64 exec, %[msk]\n" \
  "ds_read_b128 v[144:147], v153 offset:12288\n" \
  "ds_read_b128 v[148:151], v153 offset:12544\n" \
  "s_mov_b64 exec, -1\n" \
  "v_exp_f32 v82, v82\n" \
  "v_exp_f32 v83, v83\n" \
  "v_exp_f32 v84, v84\n" \
  "v_exp_f32 v85, v85\n" \
  "s_waitcnt lgkmcnt(2)\n" \
  "v_mfma_f32_32x32x16_bf16 v[64:79], v[132:135], v[128:131], v[96:111]\n" \
  "ds_read_b128 v[132:135], v152 offset:14336\n" \
  "v_exp_f32 v86, v86\n" \
  "v_exp_f32 v87, v87\n" \
  "v_exp_f32 v88, v88\n" \
  "v_exp_f32 v89, v89\n" \
  "v_exp_f32 v90, v90\n" \
  "v_smfmac_f32_32x32x32_bf16 v[112:127], v[136:139], v[80:87], v156\n" \
  "v_exp_f32 v91, v91\n" \
  "v_exp_f32 v92, v92\n" \
  "v_exp_f32 v93, v93\n" \
  "v_exp_f32 v94, v94\n" \
  "v_exp_f32 v95, v95\n" \
  "v_exp_f32 v64, v64\n" \
  "v_exp_f32 v65, v65\n" \
  "v_smfmac_f32_32x32x32_bf16 v[112:127], v[140:143], v[88:95], v156\n" \
  "s_mov_b64 exec, %[msk]\n" \
  "ds_read_b128 v[136:139], v153 offset:14336\n" \
  "ds_read_b128 v[140:143], v153 offset:14592\n" \
  "s_mov_b64 exec, -1\n" \
  "v_exp_f32 v66, v66\n" \
  "v_exp_f32 v67, v67\n" \
  "v_exp_f32 v68, v68\n" \
  "v_exp_f32 v69, v69\n" \
  "v_readfirstlane_b32 %[tmp], v155\n" \
  "s_cmp_ge_u32 %[tmp], 8\n" \
  "s_cbranch_scc1 Latt_ok%=\n" \
  "s_mov_b32 %[spin], 0x40000\n" \
  "Latt_spin%=:\n" \
  "s_sleep 1\n" \
  "ds_read_b32 v155, v154\n" \
  "s_waitcnt lgkmcnt(0)\n" \
  "v_readfirstlane_b32 %[tmp], v155\n" \
  "s_sub_u32 %[spin], %[spin], 1\n" \
  "s_cmp_eq_u32 %[spin], 0\n" \
  "s_cbranch_scc1 Latt_ok%=\n" \
  "s_cmp_lt_u32 %[tmp], 8\n" \
  "s_cbranch_scc1 Latt_spin%=\n" \
  "Latt_ok%=:\n" \
  "s_waitcnt lgkmcnt(2)\n" \
  "v_mfma_f32_32x32x16_bf16 v[80:95], v[132:135], v[128:131], v[96:111]\n" \
  "ds_read_b128 v[132:135], v152 offset:16384\n" \
  "v_exp_f32 v70, v70\n" \
  "v_exp_f32 v71, v71\n" \
  "v_exp_f32 v72, v72\n" \
  "v_exp_f32 v73, v73\n" \
  "v_exp_f32 v74, v74\n" \
  "v_smfmac_f32_32x32x32_bf16 v[112:127], v[144:147], v[64:71], v156\n" \
  "v_exp_f32 v75, v75\n" \
  "v_exp_f32 v76, v76\n" \
  "v_exp_f32 v77, v77\n" \
  "v_exp_f32 v78, v78\n" \
  "v_exp_f32 v79, v79\n" \
  "v_exp_f32 v80, v80\n" \
  "v_exp_f32 v81, v81\n" \
  "v_smfmac_f32_32x32x32_bf16 v[112:127], v[148:151], v[72:79], v156\n" \
  "s_mov_b64 exec, %[msk]\n" \
  "ds_read_b128 v[144:147], v153 offset:16384\n" \
  "ds_read_b128 v[148:151], v153 offset:16640\n" \
  "s_mov_b64 exec, -1\n" \
  "v_exp_f32 v82, v82\n" \
  "v_exp_f32 v83, v83\n" \
  "v_exp_f32 v84, v84\n" \
  "v_exp_f32 v85, v85\n" \
  "s_waitcnt lgkmcnt(2)\n" \
  "v_mfma_f32_32x32x16_bf16 v[64:79], v[132:135], v[128:131], v[96:111]\n" \
  "ds_read_b128 v[132:135], v152 offset:18432\n" \
  "v_exp_f32 v86, v86\n" \
  "v_exp_f32 v87, v87\n" \
  "v_exp_f32 v88, v88\n" \
  "v_exp_f32 v89, v89\n" \
  "v_exp_f32 v90, v90\n" \
  "v_smfmac_f32_32x32x32_bf16 v[112:127], v[136:139], v[80:87], v156\n" \
  "v_exp_f32 v91, v91\n" \
  "v_exp_f32 v92, v92\n" \
  "v_exp_f32 v93, v93\n" \
  "v_exp_f32 v94, v94\n" \
  "v_exp_f32 v95, v95\n" \
  "v_add_u32 v152, 16384, v152\n" \
  "v_add_u32 v153, 16384, v153\n" \
  "v_add_u32 v154, 4, v154\n" \
  "s_sub_u32 %[n], %[n], 1\n" \
  "s_cmp_lg_u32 %[n], 0\n" \
  "s_cbranch_scc1 Latt_loop%=\n" \
  "s_waitcnt lgkmcnt(0)\n" \
  "s_nop 7\n"

#define FFN_ASM \
  "s_mov_b32 s48, m0\n" \
  "s_mov_b64 s[36:37], %[wb]\n" \
  "s_add_u32 s38, s36, 0x2000\n" \
  "s_addc_u32 s39, s37, 0\n" \
  "s_mov_b64 s[40:41], %[a0]\n" \
  "s_mov_b64 s[42:43], %[a1]\n" \
  "s_add_u32 m0, %[sl], 0\n" \
  "s_nop 0\n" \
  "global_load_lds_dwordx4 %[wv], s[36:37]\n" \
  "s_add_u32 m0, %[sl], 1024\n" \
  "s_add_u32 s36, s36, 0x4000\n" \
  "global_load_lds_dwordx4 %[wv], s[38:39]\n" \
  "s_addc_u32 s37, s37, 0\n" \
  "s_add_u32 s38, s38, 0x4000\n" \
  "s_addc_u32 s39, s39, 0\n" \
  "s_add_u32 m0, %[sl], 2048\n" \
  "s_nop 0\n" \
  "global_load_lds_dwordx4 %[wv], s[36:37]\n" \
  "s_add_u32 m0, %[sl], 3072\n" \
  "s_add_u32 s36, s36, 0x4000\n" \
  "global_load_lds_dwordx4 %[wv], s[38:39]\n" \
  "s_addc_u32 s37, s37, 0\n" \
  "s_add_u32 s38, s38, 0x4000\n" \
  "s_addc_u32 s39, s39, 0\n" \
  "s_add_u32 m0, %[sl], 4096\n" \
  "s_nop 0\n" \
  "global_load_lds_dwordx4 %[wv], s[36:37]\n" \
  "s_add_u32 m0, %[sl], 5120\n" \
  "s_add_u32 s36, s36, 0x4000\n" \
  "global_load_lds_dwordx4 %[wv], s[38:39]\n" \
  "s_addc_u32 s37, s37, 0\n" \
  "s_add_u32 s38, s38, 0x4000\n" \
  "s_addc_u32 s39, s39, 0\n" \
  "s_mov_b32 s44, 0x4000\n" \
  "Lf_p%=:\n" \
  "global_load_dwordx2 v[100:101], %[fv], %[ws] sc1\n" \
  "s_waitcnt vmcnt(0)\n" \
  "v_cmp_ne_u64 vcc, %[tag], v[100:101]\n" \
  "s_cbranch_vccz Lf_px%=\n" \
  "s_sleep 1\n" \
  "s_sub_u32 s44, s44, 1\n" \
  "s_cmp_lg_u32 s44, 0\n" \
  "s_cbranch_scc1 Lf_p%=\n" \
  "Lf_px%=:\n" \
  "global_load_dword v102, %[gv], %[ws] sc1\n" \
  "global_load_dwordx4 v[104:107], %[l16], s[40:41] offset:0 sc1\n" \
  "global_load_dwordx4 v[108:111], %[l16], s[42:43] offset:0 sc1\n" \
  "global_load_dwordx4 v[112:115], %[l16], s[40:41] offset:1024 sc1\n" \
  "global_load_dwordx4 v[116:119], %[l16], s[42:43] offset:1024 sc1\n" \
  "global_load_dwordx4 v[120:123], %[l16], s[40:41] offset:2048 sc1\n" \
  "global_load_dwordx4 v[124:127], %[l16], s[42:43] offset:2048 sc1\n" \
  "global_load_dwordx4 v[128:131], %[l16], s[40:41] offset:3072 sc1\n" \
  "global_load_dwordx4 v[132:135], %[l16], s[42:43] offset:3072 sc1\n" \
  "s_add_u32 s40, s40, 0x1000\n" \
  "s_addc_u32 s41, s41, 0\n" \
  "s_add_u32 s42, s42, 0x1000\n" \
  "s_addc_u32 s43, s43, 0\n" \
  "global_load_dwordx4 v[136:139], %[l16], s[40:41] offset:0 sc1\n" \
  "global_load_dwordx4 v[140:143], %[l16], s[42:43] offset:0 sc1\n" \
  "ds_read_b64_tr_b16 v[144:145], %[ra] offset:0\n" \
  "ds_read_b64_tr_b16 v[146:147], %[ra] offset:512\n" \
  "ds_read_b64_tr_b16 v[148:149], %[rb] offset:0\n" \
  "ds_read_b64_tr_b16 v[150:151], %[rb] offset:512\n" \
  "s_waitcnt vmcnt(10)\n" \
  "ds_write_b32 %[gl], v102\n" \
  "s_mov_b64 exec, 1\n" \
  "v_mov_b32 v103, 2\n" \
  "ds_add_u32 %[cn], v103 offset:92\n" \
  "s_mov_b64 exec, -1\n" \
  "s_waitcnt lgkmcnt(0)\n" \
  "s_setprio 0\n" \
  "s_sleep 3\n" \
  "ds_read_b64_tr_b16 v[152:153], %[ra] offset:2048\n" \
  "ds_read_b64_tr_b16 v[154:155], %[ra] offset:2560\n" \
  "ds_read_b64_tr_b16 v[156:157], %[rb] offset:2048\n" \
  "ds_read_b64_tr_b16 v[158:159], %[rb] offset:2560\n" \
  "s_waitcnt vmcnt(8) lgkmcnt(4)\n" \
  "s_add_u32 m0, %[sl], 0\n" \
  "s_nop 0\n" \
  "global_load_lds_dwordx4 %[wv], s[36:37]\n" \
  "s_add_u32 m0, %[sl], 1024\n" \
  "s_add_u32 s36, s36, 0x4000\n" \
  "global_load_lds_dwordx4 %[wv], s[38:39]\n" \
  "s_addc_u32 s37, s37, 0\n" \
  "s_add_u32 s38, s38, 0x4000\n" \
  "s_addc_u32 s39, s39, 0\n" \
  "v_mfma_f32_32x32x16_bf16 v[0:15], v[144:147], v[104:107], v[0:15]\n" \
  "v_mfma_f32_32x32x16_bf16 v[16:31], v[144:147], v[108:111], v[16:31]\n" \
  "v_mfma_f32_32x32x16_bf16 v[32:47], v[148:151], v[104:107], v[32:47]\n" \
  "v_mfma_f32_32x32x16_bf16 v[48:63], v[148:151], v[108:111], v[48:63]\n" \
  "global_load_dwordx4 v[104:107], %[l16], s[40:41] offset:1024 sc1\n" \
  "global_load_dwordx4 v[108:111], %[l16], s[42:43] offset:1024 sc1\n" \
  "s_sleep 3\n" \
  "ds_read_b64_tr_b16 v[144:145], %[ra] offset:4096\n" \
  "ds_read_b64_tr_b16 v[146:147], %[ra] offset:4608\n" \
  "ds_read_b64_tr_b16 v[148:149], %[rb] offset:4096\n" \
  "ds_read_b64_tr_b16 v[150:151], %[rb] offset:4608\n" \
  "s_waitcnt vmcnt(10) lgkmcnt(4)\n" \
  "s_add_u32 m0, %[sl], 2048\n" \
  "s_nop 0\n" \
  "global_load_lds_dwordx4 %[wv], s[36:37]\n" \
  "s_add_u32 m0, %[sl], 3072\n" \
  "s_add_u32 s36, s36, 0x4000\n" \
  "global_load_lds_dwordx4 %[wv], s[38:39]\n" \
  "s_addc_u32 s37, s37, 0\n" \
  "s_add_u32 s38, s38, 0x4000\n" \
  "s_addc_u32 s39, s39, 0\n" \
  "v_mfma_f32_32x32x16_bf16 v[0:15], v[152:155], v[112:115], v[0:15]\n" \
  "v_mfma_f32_32x32x16_bf16 v[16:31], v[152:155], v[116:119], v[16:31]\n" \
  "v_mfma_f32_32x32x16_bf16 v[32:47], v[156:159], v[112:115], v[32:47]\n" \
  "v_mfma_f32_32x32x16_bf16 v[48:63], v[156:159], v[116:119], v[48:63]\n" \
  "global_load_dwordx4 v[112:115], %[l16], s[40:41] offset:2048 sc1\n" \
  "global_load_dwordx4 v[116:119], %[l16], s[42:43] offset:2048 sc1\n" \
  "s_sleep 3\n" \
  "s_waitcnt vmcnt(6)\n" \
  "ds_read_b64_tr_b16 v[152:153], %[ra] offset:0\n" \
  "ds_read_b64_tr_b16 v[154:155], %[ra] offset:512\n" \
  "ds_read_b64_tr_b16 v[156:157], %[rb] offset:0\n" \
  "ds_read_b64_tr_b16 v[158:159], %[rb] offset:512\n" \
  "s_waitcnt vmcnt(12) lgkmcnt(4)\n" \
  "s_add_u32 m0, %[sl], 4096\n" \
  "s_nop 0\n" \
  "global_load_lds_dwordx4 %[wv], s[36:37]\n" \
  "s_add_u32 m0, %[sl], 5120\n" \
  "s_add_u32 s36, s36, 0x4000\n" \
  "global_load_lds_dwordx4 %[wv], s[38:39]\n" \
  "s_addc_u32 s37, s37, 0\n" \
  "s_add_u32 s38, s38, 0x4000\n" \
  "s_addc_u32 s39, s39, 0\n" \
  "v_mfma_f32_32x32x16_bf16 v[0:15], v[144:147], v[120:123], v[0:15]\n" \
  "v_mfma_f32_32x32x16_bf16 v[16:31], v[144:147], v[124:127], v[16:31]\n" \
  "v_mfma_f32_32x32x16_bf16 v[32:47], v[148:151], v[120:123], v[32:47]\n" \
  "v_mfma_f32_32x32x16_bf16 v[48:63], v[148:151], v[124:127], v[48:63]\n" \
  "global_load_dwordx4 v[120:123], %[l16], s[40:41] offset:3072 sc1\n" \
  "global_load_dwordx4 v[124:127], %[l16], s[42:43] offset:3072 sc1\n" \
  "s_add_u32 s40, s40, 0x1000\n" \
  "s_addc_u32 s41, s41, 0\n" \
  "s_add_u32 s42, s42, 0x1000\n" \
  "s_addc_u32 s43, s43, 0\n" \
  "s_sleep 3\n" \
  "s_waitcnt vmcnt(6)\n" \
  "ds_read_b64_tr_b16 v[144:145], %[ra] offset:2048\n" \
  "ds_read_b64_tr_b16 v[146:147], %[ra] offset:2560\n" \
  "ds_read_b64_tr_b16 v[148:149], %[rb] offset:2048\n" \
  "ds_read_b64_tr_b16 v[150:151], %[rb] offset:2560\n" \
  "s_waitcnt vmcnt(14) lgkmcnt(4)\n" \
  "s_add_u32 m0, %[sl], 0\n" \
  "s_nop 0\n" \
  "global_load_lds_dwordx4 %[wv], s[36:37]\n" \
  "s_add_u32 m0, %[sl], 1024\n" \
  "s_add_u32 s36, s36, 0x4000\n" \
  "global_load_lds_dwordx4 %[wv], s[38:39]\n" \
  "s_addc_u32 s37, s37, 0\n" \
  "s_add_u32 s38, s38, 0x4000\n" \
  "s_addc_u32 s39, s39, 0\n" \
  "v_mfma_f32_32x32x16_bf16 v[0:15], v[152:155], v[128:131], v[0:15]\n" \
  "v_mfma_f32_32x32x16_bf16 v[16:31], v[152:155], v[132:135], v[16:31]\n" \
  "v_mfma_f32_32x32x16_bf16 v[32:47], v[156:159], v[128:131], v[32:47]\n" \
  "v_mfma_f32_32x32x16_bf16 v[48:63], v[156:159], v[132:135], v[48:63]\n" \
  "global_load_dwordx4 v[128:131], %[l16], s[40:41] offset:0 sc1\n" \
  "global_load_dwordx4 v[132:135], %[l16], s[42:43] offset:0 sc1\n" \
  "s_sleep 3\n" \
  "s_waitcnt vmcnt(6)\n" \
  "ds_read_b64_tr_b16 v[152:153], %[ra] offset:4096\n" \
  "ds_read_b64_tr_b16 v[154:155], %[ra] offset:4608\n" \
  "ds_read_b64_tr_b16 v[156:157], %[rb] offset:4096\n" \
  "ds_read_b64_tr_b16 v[158:159], %[rb] offset:4608\n" \
  "s_waitcnt vmcnt(16) lgkmcnt(4)\n" \
  "s_add_u32 m0, %[sl], 2048\n" \
  "s_nop 0\n" \
  "global_load_lds_dwordx4 %[wv], s[36:37]\n" \
  "s_add_u32 m0, %[sl], 3072\n" \
  "s_add_u32 s36, s36, 0x4000\n" \
  "global_load_lds_dwordx4 %[wv], s[38:39]\n" \
  "s_addc_u32 s37, s37, 0\n" \
  "s_add_u32 s38, s38, 0x4000\n" \
  "s_addc_u32 s39, s39, 0\n" \
  "v_mfma_f32_32x32x16_bf16 v[0:15], v[144:147], v[136:139], v[0:15]\n" \
  "v_mfma_f32_32x32x16_bf16 v[16:31], v[144:147], v[140:143], v[16:31]\n" \
  "v_mfma_f32_32x32x16_bf16 v[32:47], v[148:151], v[136:139], v[32:47]\n" \
  "v_mfma_f32_32x32x16_bf16 v[48:63], v[148:151], v[140:143], v[48:63]\n" \
  "global_load_dwordx4 v[136:139], %[l16], s[40:41] offset:1024 sc1\n" \
  "global_load_dwordx4 v[140:143], %[l16], s[42:43] offset:1024 sc1\n" \
  "s_sleep 3\n" \
  "s_waitcnt vmcnt(6)\n" \
  "ds_read_b64_tr_b16 v[144:145], %[ra] offset:0\n" \
  "ds_read_b64_tr_b16 v[146:147], %[ra] offset:512\n" \
  "ds_read_b64_tr_b16 v[148:149], %[rb] offset:0\n" \
  "ds_read_b64_tr_b16 v[150:151], %[rb] offset:512\n" \
  "s_waitcnt vmcnt(16) lgkmcnt(4)\n" \
  "s_add_u32 m0, %[sl], 4096\n" \
  "s_nop 0\n" \
  "global_load_lds_dwordx4 %[wv], s[36:37]\n" \
  "s_add_u32 m0, %[sl], 5120\n" \
  "s_add_u32 s36, s36, 0x4000\n" \
  "global_load_lds_dwordx4 %[wv], s[38:39]\n" \
  "s_addc_u32 s37, s37, 0\n" \
  "s_add_u32 s38, s38, 0x4000\n" \
  "s_addc_u32 s39, s39, 0\n" \
  "v_mfma_f32_32x32x16_bf16 v[0:15], v[152:155], v[104:107], v[0:15]\n" \
  "v_mfma_f32_32x32x16_bf16 v[16:31], v[152:155], v[108:111], v[16:31]\n" \
  "v_mfma_f32_32x32x16_bf16 v[32:47], v[156:159], v[104:107], v[32:47]\n" \
  "v_mfma_f32_32x32x16_bf16 v[48:63], v[156:159], v[108:111], v[48:63]\n" \
  "global_load_dwordx4 v[104:107], %[l16], s[40:41] offset:2048 sc1\n" \
  "global_load_dwordx4 v[108:111], %[l16], s[42:43] offset:2048 sc1\n" \
  "s_sleep 3\n" \
  "s_waitcnt vmcnt(6)\n" \
  "ds_read_b64_tr_b16 v[152:153], %[ra] offset:2048\n" \
  "ds_read_b64_tr_b16 v[154:155], %[ra] offset:2560\n" \
  "ds_read_b64_tr_b16 v[156:157], %[rb] offset:2048\n" \
  "ds_read_b64_tr_b16 v[158:159], %[rb] offset:2560\n" \
  "s_waitcnt vmcnt(16) lgkmcnt(4)\n" \
  "s_add_u32 m0, %[sl], 0\n" \
  "s_nop 0\n" \
  "global_load_lds_dwordx4 %[wv], s[36:37]\n" \
  "s_add_u32 m0, %[sl], 1024\n" \
  "s_add_u32 s36, s36, 0x4000\n" \
  "global_load_lds_dwordx4 %[wv], s[38:39]\n" \
  "s_addc_u32 s37, s37, 0\n" \
  "s_add_u32 s38, s38, 0x4000\n" \
  "s_addc_u32 s39, s39, 0\n" \
  "v_mfma_f32_32x32x16_bf16 v[0:15], v[144:147], v[112:115], v[0:15]\n" \
  "v_mfma_f32_32x32x16_bf16 v[16:31], v[144:147], v[116:119], v[16:31]\n" \
  "v_mfma_f32_32x32x16_bf16 v[32:47], v[148:151], v[112:115], v[32:47]\n" \
  "v_mfma_f32_32x32x16_bf16 v[48:63], v[148:151], v[116:119], v[48:63]\n" \
  "global_load_dwordx4 v[112:115], %[l16], s[40:41] offset:3072 sc1\n" \
  "global_load_dwordx4 v[116:119], %[l16], s[42:43] offset:3072 sc1\n" \
  "s_add_u32 s40, s40, 0x1000\n" \
  "s_addc_u32 s41, s41, 0\n" \
  "s_add_u32 s42, s42, 0x1000\n" \
  "s_addc_u32 s43, s43, 0\n" \
  "s_sleep 3\n" \
  "s_waitcnt vmcnt(6)\n" \
  "ds_read_b64_tr_b16 v[144:145], %[ra] offset:4096\n" \
  "ds_read_b64_tr_b16 v[146:147], %[ra] offset:4608\n" \
  "ds_read_b64_tr_b16 v[148:149], %[rb] offset:4096\n" \
  "ds_read_b64_tr_b16 v[150:151], %[rb] offset:4608\n" \
  "s_waitcnt vmcnt(16) lgkmcnt(4)\n" \
  "s_add_u32 m0, %[sl], 2048\n" \
  "s_nop 0\n" \
  "global_load_lds_dwordx4 %[wv], s[36:37]\n" \
  "s_add_u32 m0, %[sl], 3072\n" \
  "s_add_u32 s36, s36, 0x4000\n" \
  "global_load_lds_dwordx4 %[wv], s[38:39]\n" \
  "s_addc_u32 s37, s37, 0\n" \
  "s_add_u32 s38, s38, 0x4000\n" \
  "s_addc_u32 s39, s39, 0\n" \
  "v_mfma_f32_32x32x16_bf16 v[0:15], v[152:155], v[120:123], v[0:15]\n" \
  "v_mfma_f32_32x32x16_bf16 v[16:31], v[152:155], v[124:127], v[16:31]\n" \
  "v_mfma_f32_32x32x16_bf16 v[32:47], v[156:159], v[120:123], v[32:47]\n" \
  "v_mfma_f32_32x32x16_bf16 v[48:63], v[156:159], v[124:127], v[48:63]\n" \
  "global_load_dwordx4 v[120:123], %[l16], s[40:41] offset:0 sc1\n" \
  "global_load_dwordx4 v[124:127], %[l16], s[42:43] offset:0 sc1\n" \
  "s_sleep 3\n" \
  "s_waitcnt vmcnt(6)\n" \
  "ds_read_b64_tr_b16 v[152:153], %[ra] offset:0\n" \
  "ds_read_b64_tr_b16 v[154:155], %[ra] offset:512\n" \
  "ds_read_b64_tr_b16 v[156:157], %[rb] offset:0\n" \
  "ds_read_b64_tr_b16 v[158:159], %[rb] offset:512\n" \
  "s_waitcnt vmcnt(16) lgkmcnt(4)\n" \
  "s_add_u32 m0, %[sl], 4096\n" \
  "s_nop 0\n" \
  "global_load_lds_dwordx4 %[wv], s[36:37]\n" \
  "s_add_u32 m0, %[sl], 5120\n" \
  "s_add_u32 s36, s36, 0x4000\n" \
  "global_load_lds_dwordx4 %[wv], s[38:39]\n" \
  "s_addc_u32 s37, s37, 0\n" \
  "s_add_u32 s38, s38, 0x4000\n" \
  "s_addc_u32 s39, s39, 0\n" \
  "v_mfma_f32_32x32x16_bf16 v[0:15], v[144:147], v[128:131], v[0:15]\n" \
  "v_mfma_f32_32x32x16_bf16 v[16:31], v[144:147], v[132:135], v[16:31]\n" \
  "v_mfma_f32_32x32x16_bf16 v[32:47], v[148:151], v[128:131], v[32:47]\n" \
  "v_mfma_f32_32x32x16_bf16 v[48:63], v[148:151], v[132:135], v[48:63]\n" \
  "global_load_dwordx4 v[128:131], %[l16], s[40:41] offset:1024 sc1\n" \
  "global_load_dwordx4 v[132:135], %[l16], s[42:43] offset:1024 sc1\n" \
  "s_sleep 3\n" \
  "s_waitcnt vmcnt(6)\n" \
  "ds_read_b64_tr_b16 v[144:145], %[ra] offset:2048\n" \
  "ds_read_b64_tr_b16 v[146:147], %[ra] offset:2560\n" \
  "ds_read_b64_tr_b16 v[148:149], %[rb] offset:2048\n" \
  "ds_read_b64_tr_b16 v[150:151], %[rb] offset:2560\n" \
  "s_waitcnt vmcnt(16) lgkmcnt(4)\n" \
  "s_add_u32 m0, %[sl], 0\n" \
  "s_nop 0\n" \
  "global_load_lds_dwordx4 %[wv], s[36:37]\n" \
  "s_add_u32 m0, %[sl], 1024\n" \
  "s_add_u32 s36, s36, 0x4000\n" \
  "global_load_lds_dwordx4 %[wv], s[38:39]\n" \
  "s_addc_u32 s37, s37, 0\n" \
  "s_add_u32 s38, s38, 0x4000\n" \
  "s_addc_u32 s39, s39, 0\n" \
  "v_mfma_f32_32x32x16_bf16 v[0:15], v[152:155], v[136:139], v[0:15]\n" \
  "v_mfma_f32_32x32x16_bf16 v[16:31], v[152:155], v[140:143], v[16:31]\n" \
  "v_mfma_f32_32x32x16_bf16 v[32:47], v[156:159], v[136:139], v[32:47]\n" \
  "v_mfma_f32_32x32x16_bf16 v[48:63], v[156:159], v[140:143], v[48:63]\n" \
  "global_load_dwordx4 v[136:139], %[l16], s[40:41] offset:2048 sc1\n" \
  "global_load_dwordx4 v[140:143], %[l16], s[42:43] offset:2048 sc1\n" \
  "s_sleep 3\n" \
  "s_waitcnt vmcnt(6)\n" \
  "ds_read_b64_tr_b16 v[152:153], %[ra] offset:4096\n" \
  "ds_read_b64_tr_b16 v[154:155], %[ra] offset:4608\n" \
  "ds_read_b64_tr_b16 v[156:157], %[rb] offset:4096\n" \
  "ds_read_b64_tr_b16 v[158:159], %[rb] offset:4608\n" \
  "s_waitcnt vmcnt(16) lgkmcnt(4)\n" \
  "s_add_u32 m0, %[sl], 2048\n" \
  "s_nop 0\n" \
  "global_load_lds_dwordx4 %[wv], s[36:37]\n" \
  "s_add_u32 m0, %[sl], 3072\n" \
  "s_add_u32 s36, s36, 0x4000\n" \
  "global_load_lds_dwordx4 %[wv], s[38:39]\n" \
  "s_addc_u32 s37, s37, 0\n" \
  "s_add_u32 s38, s38, 0x4000\n" \
  "s_addc_u32 s39, s39, 0\n" \
  "v_mfma_f32_32x32x16_bf16 v[0:15], v[144:147], v[104:107], v[0:15]\n" \
  "v_mfma_f32_32x32x16_bf16 v[16:31], v[144:147], v[108:111], v[16:31]\n" \
  "v_mfma_f32_32x32x16_bf16 v[32:47], v[148:151], v[104:107], v[32:47]\n" \
  "v_mfma_f32_32x32x16_bf16 v[48:63], v[148:151], v[108:111], v[48:63]\n" \
  "global_load_dwordx4 v[104:107], %[l16], s[40:41] offset:3072 sc1\n" \
  "global_load_dwordx4 v[108:111], %[l16], s[42:43] offset:3072 sc1\n" \
  "s_add_u32 s40, s40, 0x1000\n" \
  "s_addc_u32 s41, s41, 0\n" \
  "s_add_u32 s42, s42, 0x1000\n" \
  "s_addc_u32 s43, s43, 0\n" \
  "s_sleep 3\n" \
  "s_waitcnt vmcnt(6)\n" \
  "ds_read_b64_tr_b16 v[144:145], %[ra] offset:0\n" \
  "ds_read_b64_tr_b16 v[146:147], %[ra] offset:512\n" \
  "ds_read_b64_tr_b16 v[148:149], %[rb] offset:0\n" \
  "ds_read_b64_tr_b16 v[150:151], %[rb] offset:512\n" \
  "s_waitcnt vmcnt(16) lgkmcnt(4)\n" \
  "s_add_u32 m0, %[sl], 4096\n" \
  "s_nop 0\n" \
  "global_load_lds_dwordx4 %[wv], s[36:37]\n" \
  "s_add_u32 m0, %[sl], 5120\n" \
  "s_add_u32 s36, s36, 0x4000\n" \
  "global_load_lds_dwordx4 %[wv], s[38:39]\n" \
  "s_addc_u32 s37, s37, 0\n" \
  "s_add_u32 s38, s38, 0x4000\n" \
  "s_addc_u32 s39, s39, 0\n" \
  "v_mfma_f32_32x32x16_bf16 v[0:15], v[152:155], v[112:115], v[0:15]\n" \
  "v_mfma_f32_32x32x16_bf16 v[16:31], v[152:155], v[116:119], v[16:31]\n" \
  "v_mfma_f32_32x32x16_bf16 v[32:47], v[156:159], v[112:115], v[32:47]\n" \
  "v_mfma_f32_32x32x16_bf16 v[48:63], v[156:159], v[116:119], v[48:63]\n" \
  "global_load_dwordx4 v[112:115], %[l16], s[40:41] offset:0 sc1\n" \
  "global_load_dwordx4 v[116:119], %[l16], s[42:43] offset:0 sc1\n" \
  "s_sleep 3\n" \
  "s_waitcnt vmcnt(6)\n" \
  "ds_read_b64_tr_b16 v[152:153], %[ra] offset:2048\n" \
  "ds_read_b64_tr_b16 v[154:155], %[ra] offset:2560\n" \
  "ds_read_b64_tr_b16 v[156:157], %[rb] offset:2048\n" \
  "ds_read_b64_tr_b16 v[158:159], %[rb] offset:2560\n" \
  "s_waitcnt vmcnt(16) lgkmcnt(4)\n" \
  "s_add_u32 m0, %[sl], 0\n" \
  "s_nop 0\n" \
  "global_load_lds_dwordx4 %[wv], s[36:37]\n" \
  "s_add_u32 m0, %[sl], 1024\n" \
  "s_add_u32 s36, s36, 0x4000\n" \
  "global_load_lds_dwordx4 %[wv], s[38:39]\n" \
  "s_addc_u32 s37, s37, 0\n" \
  "s_add_u32 s38, s38, 0x4000\n" \
  "s_addc_u32 s39, s39, 0\n" \
  "v_mfma_f32_32x32x16_bf16 v[0:15], v[144:147], v[120:123], v[0:15]\n" \
  "v_mfma_f32_32x32x16_bf16 v[16:31], v[144:147], v[124:127], v[16:31]\n" \
  "v_mfma_f32_32x32x16_bf16 v[32:47], v[148:151], v[120:123], v[32:47]\n" \
  "v_mfma_f32_32x32x16_bf16 v[48:63], v[148:151], v[124:127], v[48:63]\n" \
  "global_load_dwordx4 v[120:123], %[l16], s[40:41] offset:1024 sc1\n" \
  "global_load_dwordx4 v[124:127], %[l16], s[42:43] offset:1024 sc1\n" \
  "s_sleep 3\n" \
  "s_setprio 3\n" \
  "s_mov_b32 s44, 0x40000\n" \
  "Lf_h%=:\n" \
  "ds_read_b32 v100, %[cn] offset:4\n" \
  "s_waitcnt lgkmcnt(0)\n" \
  "v_readfirstlane_b32 s45, v100\n" \
  "s_cmp_ge_u32 s45, 8\n" \
  "s_cbranch_scc1 Lf_hx%=\n" \
  "s_sleep 8\n" \
  "s_sub_u32 s44, s44, 1\n" \
  "s_cmp_lg_u32 s44, 0\n" \
  "s_cbranch_scc1 Lf_h%=\n" \
  "Lf_hx%=:\n" \
  "s_mov_b64 s[46:47], %[wo]\n" \
  "s_mov_b32 s45, %[wl]\n" \
  "s_mov_b32 m0, s45\n" \
  "s_add_u32 s45, s45, 0x400\n" \
  "global_load_lds_dwordx4 %[wv], s[46:47]\n" \
  "s_add_u32 s46, s46, 0x2000\n" \
  "s_addc_u32 s47, s47, 0\n" \
  "s_mov_b32 m0, s45\n" \
  "s_add_u32 s45, s45, 0x400\n" \
  "global_load_lds_dwordx4 %[wv], s[46:47]\n" \
  "s_add_u32 s46, s46, 0x2000\n" \
  "s_addc_u32 s47, s47, 0\n" \
  "s_mov_b32 m0, s45\n" \
  "s_add_u32 s45, s45, 0x400\n" \
  "global_load_lds_dwordx4 %[wv], s[46:47]\n" \
  "s_add_u32 s46, s46, 0x2000\n" \
  "s_addc_u32 s47, s47, 0\n" \
  "s_mov_b32 m0, s45\n" \
  "s_add_u32 s45, s45, 0x400\n" \
  "global_load_lds_dwordx4 %[wv], s[46:47]\n" \
  "s_add_u32 s46, s46, 0x2000\n" \
  "s_addc_u32 s47, s47, 0\n" \
  "s_mov_b32 m0, s45\n" \
  "s_add_u32 s45, s45, 0x400\n" \
  "global_load_lds_dwordx4 %[wv], s[46:47]\n" \
  "s_add_u32 s46, s46, 0x2000\n" \
  "s_addc_u32 s47, s47, 0\n" \
  "s_mov_b32 m0, s45\n" \
  "s_add_u32 s45, s45, 0x400\n" \
  "global_load_lds_dwordx4 %[wv], s[46:47]\n" \
  "s_add_u32 s46, s46, 0x2000\n" \
  "s_addc_u32 s47, s47, 0\n" \
  "s_mov_b32 m0, s45\n" \
  "s_add_u32 s45, s45, 0x400\n" \
  "global_load_lds_dwordx4 %[wv], s[46:47]\n" \
  "s_add_u32 s46, s46, 0x2000\n" \
  "s_addc_u32 s47, s47, 0\n" \
  "s_mov_b32 m0, s45\n" \
  "s_add_u32 s45, s45, 0x400\n" \
  "global_load_lds_dwordx4 %[wv], s[46:47]\n" \
  "s_add_u32 s46, s46, 0x2000\n" \
  "s_addc_u32 s47, s47, 0\n" \
  "s_mov_b32 m0, s45\n" \
  "s_add_u32 s45, s45, 0x400\n" \
  "global_load_lds_dwordx4 %[wv], s[46:47]\n" \
  "s_add_u32 s46, s46, 0x2000\n" \
  "s_addc_u32 s47, s47, 0\n" \
  "s_mov_b32 m0, s45\n" \
  "s_add_u32 s45, s45, 0x400\n" \
  "global_load_lds_dwordx4 %[wv], s[46:47]\n" \
  "s_add_u32 s46, s46, 0x2000\n" \
  "s_addc_u32 s47, s47, 0\n" \
  "s_mov_b32 m0, s45\n" \
  "s_add_u32 s45, s45, 0x400\n" \
  "global_load_lds_dwordx4 %[wv], s[46:47]\n" \
  "s_add_u32 s46, s46, 0x2000\n" \
  "s_addc_u32 s47, s47, 0\n" \
  "s_mov_b32 m0, s45\n" \
  "s_add_u32 s45, s45, 0x400\n" \
  "global_load_lds_dwordx4 %[wv], s[46:47]\n" \
  "s_add_u32 s46, s46, 0x2000\n" \
  "s_addc_u32 s47, s47, 0\n" \
  "s_mov_b32 m0, s45\n" \
  "s_add_u32 s45, s45, 0x400\n" \
  "global_load_lds_dwordx4 %[wv], s[46:47]\n" \
  "s_add_u32 s46, s46, 0x2000\n" \
  "s_addc_u32 s47, s47, 0\n" \
  "s_mov_b32 m0, s45\n" \
  "s_add_u32 s45, s45, 0x400\n" \
  "global_load_lds_dwordx4 %[wv], s[46:47]\n" \
  "s_add_u32 s46, s46, 0x2000\n" \
  "s_addc_u32 s47, s47, 0\n" \
  "s_mov_b32 m0, s45\n" \
  "s_add_u32 s45, s45, 0x400\n" \
  "global_load_lds_dwordx4 %[wv], s[46:47]\n" \
  "s_add_u32 s46, s46, 0x2000\n" \
  "s_addc_u32 s47, s47, 0\n" \
  "s_mov_b32 m0, s45\n" \
  "s_add_u32 s45, s45, 0x400\n" \
  "global_load_lds_dwordx4 %[wv], s[46:47]\n" \
  "s_add_u32 s46, s46, 0x2000\n" \
  "s_addc_u32 s47, s47, 0\n" \
  "s_mov_b32 s44, 0x4000\n" \
  "Lf_fo%=:\n" \
  "global_load_dwordx2 v[100:101], %[fo], %[ws] sc1\n" \
  "s_waitcnt vmcnt(0)\n" \
  "v_cmp_ne_u64 vcc, %[tag], v[100:101]\n" \
  "s_cbranch_vccz Lf_fox%=\n" \
  "s_sleep 1\n" \
  "s_sub_u32 s44, s44, 1\n" \
  "s_cmp_lg_u32 s44, 0\n" \
  "s_cbranch_scc1 Lf_fo%=\n" \
  "Lf_fox%=:\n" \
  "s_mov_b64 exec, 1\n" \
  "v_mov_b32 v103, 1\n" \
  "ds_add_u32 %[cn], v103 offset:12\n" \
  "s_mov_b64 exec, -1\n" \
  "s_waitcnt lgkmcnt(0)\n" \
  "s_setprio 0\n" \
  "s_waitcnt vmcnt(6)\n" \
  "ds_read_b64_tr_b16 v[144:145], %[ra] offset:4096\n" \
  "ds_read_b64_tr_b16 v[146:147], %[ra] offset:4608\n" \
  "ds_read_b64_tr_b16 v[148:149], %[rb] offset:4096\n" \
  "ds_read_b64_tr_b16 v[150:151], %[rb] offset:4608\n" \
  "s_waitcnt vmcnt(16) lgkmcnt(4)\n" \
  "s_add_u32 m0, %[sl], 2048\n" \
  "s_nop 0\n" \
  "global_load_lds_dwordx4 %[wv], s[36:37]\n" \
  "s_add_u32 m0, %[sl], 3072\n" \
  "s_add_u32 s36, s36, 0x4000\n" \
  "global_load_lds_dwordx4 %[wv], s[38:39]\n" \
  "s_addc_u32 s37, s37, 0\n" \
  "s_add_u32 s38, s38, 0x4000\n" \
  "s_addc_u32 s39, s39, 0\n" \
  "v_mfma_f32_32x32x16_bf16 v[0:15], v[152:155], v[128:131], v[0:15]\n" \
  "v_mfma_f32_32x32x16_bf16 v[16:31], v[152:155], v[132:135], v[16:31]\n" \
  "v_mfma_f32_32x32x16_bf16 v[32:47], v[156:159], v[128:131], v[32:47]\n" \
  "v_mfma_f32_32x32x16_bf16 v[48:63], v[156:159], v[132:135], v[48:63]\n" \
  "global_load_dwordx4 v[128:131], %[l16], s[40:41] offset:2048 sc1\n" \
  "global_load_dwordx4 v[132:135], %[l16], s[42:43] offset:2048 sc1\n" \
  "s_sleep 3\n" \
  "s_waitcnt vmcnt(6)\n" \
  "ds_read_b64_tr_b16 v[152:153], %[ra] offset:0\n" \
  "ds_read_b64_tr_b16 v[154:155], %[ra] offset:512\n" \
  "ds_read_b64_tr_b16 v[156:157], %[rb] offset:0\n" \
  "ds_read_b64_tr_b16 v[158:159], %[rb] offset:512\n" \
  "s_waitcnt vmcnt(16) lgkmcnt(4)\n" \
  "s_add_u32 m0, %[sl], 4096\n" \
  "s_nop 0\n" \
  "global_load_lds_dwordx4 %[wv], s[36:37]\n" \
  "s_add_u32 m0, %[sl], 5120\n" \
  "s_add_u32 s36, s36, 0x4000\n" \
  "global_load_lds_dwordx4 %[wv], s[38:39]\n" \
  "s_addc_u32 s37, s37, 0\n" \
  "s_add_u32 s38, s38, 0x4000\n" \
  "s_addc_u32 s39, s39, 0\n" \
  "v_mfma_f32_32x32x16_bf16 v[0:15], v[144:147], v[136:139], v[0:15]\n" \
  "v_mfma_f32_32x32x16_bf16 v[16:31], v[144:147], v[140:143], v[16:31]\n" \
  "v_mfma_f32_32x32x16_bf16 v[32:47], v[148:151], v[136:139], v[32:47]\n" \
  "v_mfma_f32_32x32x16_bf16 v[48:63], v[148:151], v[140:143], v[48:63]\n" \
  "global_load_dwordx4 v[136:139], %[l16], s[40:41] offset:3072 sc1\n" \
  "global_load_dwordx4 v[140:143], %[l16], s[42:43] offset:3072 sc1\n" \
  "s_add_u32 s40, s40, 0x1000\n" \
  "s_addc_u32 s41, s41, 0\n" \
  "s_add_u32 s42, s42, 0x1000\n" \
  "s_addc_u32 s43, s43, 0\n" \
  "s_sleep 3\n" \
  "s_waitcnt vmcnt(6)\n" \
  "ds_read_b64_tr_b16 v[144:145], %[ra] offset:2048\n" \
  "ds_read_b64_tr_b16 v[146:147], %[ra] offset:2560\n" \
  "ds_read_b64_tr_b16 v[148:149], %[rb] offset:2048\n" \
  "ds_read_b64_tr_b16 v[150:151], %[rb] offset:2560\n" \
  "s_waitcnt vmcnt(16) lgkmcnt(4)\n" \
  "s_add_u32 m0, %[sl], 0\n" \
  "s_nop 0\n" \
  "global_load_lds_dwordx4 %[wv], s[36:37]\n" \
  "s_add_u32 m0, %[sl], 1024\n" \
  "s_add_u32 s36, s36, 0x4000\n" \
  "global_load_lds_dwordx4 %[wv], s[38:39]\n" \
  "s_addc_u32 s37, s37, 0\n" \
  "s_add_u32 s38, s38, 0x4000\n" \
  "s_addc_u32 s39, s39, 0\n" \
  "v_mfma_f32_32x32x16_bf16 v[0:15], v[152:155], v[104:107], v[0:15]\n" \
  "v_mfma_f32_32x32x16_bf16 v[16:31], v[152:155], v[108:111], v[16:31]\n" \
  "v_mfma_f32_32x32x16_bf16 v[32:47], v[156:159], v[104:107], v[32:47]\n" \
  "v_mfma_f32_32x32x16_bf16 v[48:63], v[156:159], v[108:111], v[48:63]\n" \
  "global_load_dwordx4 v[104:107], %[l16], s[40:41] offset:0 sc1\n" \
  "global_load_dwordx4 v[108:111], %[l16], s[42:43] offset:0 sc1\n" \
  "s_sleep 3\n" \
  "s_waitcnt vmcnt(6)\n" \
  "ds_read_b64_tr_b16 v[152:153], %[ra] offset:4096\n" \
  "ds_read_b64_tr_b16 v[154:155], %[ra] offset:4608\n" \
  "ds_read_b64_tr_b16 v[156:157], %[rb] offset:4096\n" \
  "ds_read_b64_tr_b16 v[158:159], %[rb] offset:4608\n" \
  "s_waitcnt vmcnt(16) lgkmcnt(4)\n" \
  "s_add_u32 m0, %[sl], 2048\n" \
  "s_nop 0\n" \
  "global_load_lds_dwordx4 %[wv], s[36:37]\n" \
  "s_add_u32 m0, %[sl], 3072\n" \
  "s_add_u32 s36, s36, 0x4000\n" \
  "global_load_lds_dwordx4 %[wv], s[38:39]\n" \
  "s_addc_u32 s37, s37, 0\n" \
  "s_add_u32 s38, s38, 0x4000\n" \
  "s_addc_u32 s39, s39, 0\n" \
  "v_mfma_f32_32x32x16_bf16 v[0:15], v[144:147], v[112:115], v[0:15]\n" \
  "v_mfma_f32_32x32x16_bf16 v[16:31], v[144:147], v[116:119], v[16:31]\n" \
  "v_mfma_f32_32x32x16_bf16 v[32:47], v[148:151], v[112:115], v[32:47]\n" \
  "v_mfma_f32_32x32x16_bf16 v[48:63], v[148:151], v[116:119], v[48:63]\n" \
  "global_load_dwordx4 v[112:115], %[l16], s[40:41] offset:1024 sc1\n" \
  "global_load_dwordx4 v[116:119], %[l16], s[42:43] offset:1024 sc1\n" \
  "s_sleep 3\n" \
  "s_waitcnt vmcnt(6)\n" \
  "ds_read_b64_tr_b16 v[144:145], %[ra] offset:0\n" \
  "ds_read_b64_tr_b16 v[146:147], %[ra] offset:512\n" \
  "ds_read_b64_tr_b16 v[148:149], %[rb] offset:0\n" \
  "ds_read_b64_tr_b16 v[150:151], %[rb] offset:512\n" \
  "s_waitcnt vmcnt(16) lgkmcnt(4)\n" \
  "s_add_u32 m0, %[sl], 4096\n" \
  "s_nop 0\n" \
  "global_load_lds_dwordx4 %[wv], s[36:37]\n" \
  "s_add_u32 m0, %[sl], 5120\n" \
  "s_add_u32 s36, s36, 0x4000\n" \
  "global_load_lds_dwordx4 %[wv], s[38:39]\n" \
  "s_addc_u32 s37, s37, 0\n" \
  "s_add_u32 s38, s38, 0x4000\n" \
  "s_addc_u32 s39, s39, 0\n" \
  "v_mfma_f32_32x32x16_bf16 v[0:15], v[152:155], v[120:123], v[0:15]\n" \
  "v_mfma_f32_32x32x16_bf16 v[16:31], v[152:155], v[124:127], v[16:31]\n" \
  "v_mfma_f32_32x32x16_bf16 v[32:47], v[156:159], v[120:123], v[32:47]\n" \
  "v_mfma_f32_32x32x16_bf16 v[48:63], v[156:159], v[124:127], v[48:63]\n" \
  "global_load_dwordx4 v[120:123], %[l16], s[40:41] offset:2048 sc1\n" \
  "global_load_dwordx4 v[124:127], %[l16], s[42:43] offset:2048 sc1\n" \
  "s_sleep 3\n" \
  "s_waitcnt vmcnt(6)\n" \
  "ds_read_b64_tr_b16 v[152:153], %[ra] offset:2048\n" \
  "ds_read_b64_tr_b16 v[154:155], %[ra] offset:2560\n" \
  "ds_read_b64_tr_b16 v[156:157], %[rb] offset:2048\n" \
  "ds_read_b64_tr_b16 v[158:159], %[rb] offset:2560\n" \
  "s_waitcnt vmcnt(16) lgkmcnt(4)\n" \
  "s_add_u32 m0, %[sl], 0\n" \
  "s_nop 0\n" \
  "global_load_lds_dwordx4 %[wv], s[36:37]\n" \
  "s_add_u32 m0, %[sl], 1024\n" \
  "s_add_u32 s36, s36, 0x4000\n" \
  "global_load_lds_dwordx4 %[wv], s[38:39]\n" \
  "s_addc_u32 s37, s37, 0\n" \
  "s_add_u32 s38, s38, 0x4000\n" \
  "s_addc_u32 s39, s39, 0\n" \
  "v_mfma_f32_32x32x16_bf16 v[0:15], v[144:147], v[128:131], v[0:15]\n" \
  "v_mfma_f32_32x32x16_bf16 v[16:31], v[144:147], v[132:135], v[16:31]\n" \
  "v_mfma_f32_32x32x16_bf16 v[32:47], v[148:151], v[128:131], v[32:47]\n" \
  "v_mfma_f32_32x32x16_bf16 v[48:63], v[148:151], v[132:135], v[48:63]\n" \
  "global_load_dwordx4 v[128:131], %[l16], s[40:41] offset:3072 sc1\n" \
  "global_load_dwordx4 v[132:135], %[l16], s[42:43] offset:3072 sc1\n" \
  "s_add_u32 s40, s40, 0x1000\n" \
  "s_addc_u32 s41, s41, 0\n" \
  "s_add_u32 s42, s42, 0x1000\n" \
  "s_addc_u32 s43, s43, 0\n" \
  "s_sleep 3\n" \
  "s_waitcnt vmcnt(6)\n" \
  "ds_read_b64_tr_b16 v[144:145], %[ra] offset:4096\n" \
  "ds_read_b64_tr_b16 v[146:147], %[ra] offset:4608\n" \
  "ds_read_b64_tr_b16 v[148:149], %[rb] offset:4096\n" \
  "ds_read_b64_tr_b16 v[150:151], %[rb] offset:4608\n" \
  "s_waitcnt vmcnt(16) lgkmcnt(4)\n" \
  "s_add_u32 m0, %[sl], 2048\n" \
  "s_nop 0\n" \
  "global_load_lds_dwordx4 %[wv], s[36:37]\n" \
  "s_add_u32 m0, %[sl], 3072\n" \
  "s_add_u32 s36, s36, 0x4000\n" \
  "global_load_lds_dwordx4 %[wv], s[38:39]\n" \
  "s_addc_u32 s37, s37, 0\n" \
  "s_add_u32 s38, s38, 0x4000\n" \
  "s_addc_u32 s39, s39, 0\n" \
  "v_mfma_f32_32x32x16_bf16 v[0:15], v[152:155], v[136:139], v[0:15]\n" \
  "v_mfma_f32_32x32x16_bf16 v[16:31], v[152:155], v[140:143], v[16:31]\n" \
  "v_mfma_f32_32x32x16_bf16 v[32:47], v[156:159], v[136:139], v[32:47]\n" \
  "v_mfma_f32_32x32x16_bf16 v[48:63], v[156:159], v[140:143], v[48:63]\n" \
  "global_load_dwordx4 v[136:139], %[l16], s[40:41] offset:0 sc1\n" \
  "global_load_dwordx4 v[140:143], %[l16], s[42:43] offset:0 sc1\n" \
  "s_add_u32 m0, %[xl], 0\n" \
  "s_nop 0\n" \
  "global_load_lds_dwordx4 %[wv], s[36:37]\n" \
  "s_add_u32 m0, %[xl], 1024\n" \
  "s_add_u32 s36, s36, 0x4000\n" \
  "global_load_lds_dwordx4 %[wv], s[38:39]\n" \
  "s_addc_u32 s37, s37, 0\n" \
  "s_add_u32 s38, s38, 0x4000\n" \
  "s_addc_u32 s39, s39, 0\n" \
  "s_add_u32 m0, %[xl], 2048\n" \
  "s_nop 0\n" \
  "global_load_lds_dwordx4 %[wv], s[36:37]\n" \
  "s_add_u32 m0, %[xl], 3072\n" \
  "s_add_u32 s36, s36, 0x4000\n" \
  "global_load_lds_dwordx4 %[wv], s[38:39]\n" \
  "s_addc_u32 s37, s37, 0\n" \
  "s_add_u32 s38, s38, 0x4000\n" \
  "s_addc_u32 s39, s39, 0\n" \
  "global_load_dwordx4 v[72:75], %[l16], s[40:41] offset:1024 sc1\n" \
  "global_load_dwordx4 v[76:79], %[l16], s[42:43] offset:1024 sc1\n" \
  "global_load_dwordx4 v[80:83], %[l16], s[40:41] offset:2048 sc1\n" \
  "global_load_dwordx4 v[84:87], %[l16], s[42:43] offset:2048 sc1\n" \
  "global_load_dwordx4 v[88:91], %[l16], s[40:41] offset:3072 sc1\n" \
  "global_load_dwordx4 v[92:95], %[l16], s[42:43] offset:3072 sc1\n" \
  "s_add_u32 s40, s40, 0x1000\n" \
  "s_addc_u32 s41, s41, 0\n" \
  "s_add_u32 s42, s42, 0x1000\n" \
  "s_addc_u32 s43, s43, 0\n" \
  "s_waitcnt vmcnt(12)\n" \
  "ds_read_b64_tr_b16 v[152:153], %[ra] offset:0\n" \
  "ds_read_b64_tr_b16 v[154:155], %[ra] offset:512\n" \
  "ds_read_b64_tr_b16 v[156:157], %[rb] offset:0\n" \
  "ds_read_b64_tr_b16 v[158:159], %[rb] offset:512\n" \
  "ds_read_b64_tr_b16 v[64:65], %[ra] offset:2048\n" \
  "ds_read_b64_tr_b16 v[66:67], %[ra] offset:2560\n" \
  "ds_read_b64_tr_b16 v[68:69], %[rb] offset:2048\n" \
  "ds_read_b64_tr_b16 v[70:71], %[rb] offset:2560\n" \
  "s_setprio 3\n" \
  "s_sub_u32 s45, %[xl], %[sl]\n" \
  "v_add_u32 v102, s45, %[ra]\n" \
  "v_add_u32 v103, s45, %[rb]\n" \
  "s_waitcnt lgkmcnt(0)\n" \
  "s_add_u32 m0, %[sl], 4096\n" \
  "s_nop 0\n" \
  "global_load_lds_dwordx4 %[wv], s[36:37]\n" \
  "s_add_u32 m0, %[sl], 5120\n" \
  "s_add_u32 s36, s36, 0x4000\n" \
  "global_load_lds_dwordx4 %[wv], s[38:39]\n" \
  "s_addc_u32 s37, s37, 0\n" \
  "s_add_u32 s38, s38, 0x4000\n" \
  "s_addc_u32 s39, s39, 0\n" \
  "s_add_u32 m0, %[sl], 0\n" \
  "s_nop 0\n" \
  "global_load_lds_dwordx4 %[wv], s[36:37]\n" \
  "s_add_u32 m0, %[sl], 1024\n" \
  "s_add_u32 s36, s36, 0x4000\n" \
  "global_load_lds_dwordx4 %[wv], s[38:39]\n" \
  "s_addc_u32 s37, s37, 0\n" \
  "s_add_u32 s38, s38, 0x4000\n" \
  "s_addc_u32 s39, s39, 0\n" \
  "s_add_u32 m0, %[sl], 2048\n" \
  "s_nop 0\n" \
  "global_load_lds_dwordx4 %[wv], s[36:37]\n" \
  "s_add_u32 m0, %[sl], 3072\n" \
  "s_add_u32 s36, s36, 0x4000\n" \
  "global_load_lds_dwordx4 %[wv], s[38:39]\n" \
  "s_addc_u32 s37, s37, 0\n" \
  "s_add_u32 s38, s38, 0x4000\n" \
  "s_addc_u32 s39, s39, 0\n" \
  "s_mov_b32 m0, s48\n" \
  "s_mov_b32 s44, 0x40000\n" \
  "Lf_e%=:\n" \
  "ds_read_b32 v100, %[cn] offset:8\n" \
  "s_waitcnt lgkmcnt(0)\n" \
  "v_readfirstlane_b32 s45, v100\n" \
  "s_cmp_ge_u32 s45, 8\n" \
  "s_cbranch_scc1 Lf_ex%=\n" \
  "s_sleep 2\n" \
  "s_sub_u32 s44, s44, 1\n" \
  "s_cmp_lg_u32 s44, 0\n" \
  "s_cbranch_scc1 Lf_e%=\n" \
  "Lf_ex%=:\n" \
  "s_waitcnt vmcnt(0)\n" \
  "v_mfma_f32_32x32x16_bf16 v[0:15], v[144:147], v[104:107], v[0:15]\n" \
  "v_mfma_f32_32x32x16_bf16 v[16:31], v[144:147], v[108:111], v[16:31]\n" \
  "v_mfma_f32_32x32x16_bf16 v[32:47], v[148:151], v[104:107], v[32:47]\n" \
  "v_mfma_f32_32x32x16_bf16 v[48:63], v[148:151], v[108:111], v[48:63]\n" \
  "ds_read_b64_tr_b16 v[144:145], v102 offset:0\n" \
  "ds_read_b64_tr_b16 v[146:147], v102 offset:512\n" \
  "ds_read_b64_tr_b16 v[148:149], v103 offset:0\n" \
  "ds_read_b64_tr_b16 v[150:151], v103 offset:512\n" \
  "v_mfma_f32_32x32x16_bf16 v[0:15], v[152:155], v[112:115], v[0:15]\n" \
  "v_mfma_f32_32x32x16_bf16 v[16:31], v[152:155], v[116:119], v[16:31]\n" \
  "v_mfma_f32_32x32x16_bf16 v[32:47], v[156:159], v[112:115], v[32:47]\n" \
  "v_mfma_f32_32x32x16_bf16 v[48:63], v[156:159], v[116:119], v[48:63]\n" \
  "ds_read_b64_tr_b16 v[152:153], v102 offset:2048\n" \
  "ds_read_b64_tr_b16 v[154:155], v102 offset:2560\n" \
  "ds_read_b64_tr_b16 v[156:157], v103 offset:2048\n" \
  "ds_read_b64_tr_b16 v[158:159], v103 offset:2560\n" \
  "v_mfma_f32_32x32x16_bf16 v[0:15], v[64:67], v[120:123], v[0:15]\n" \
  "v_mfma_f32_32x32x16_bf16 v[16:31], v[64:67], v[124:127], v[16:31]\n" \
  "v_mfma_f32_32x32x16_bf16 v[32:47], v[68:71], v[120:123], v[32:47]\n" \
  "v_mfma_f32_32x32x16_bf16 v[48:63], v[68:71], v[124:127], v[48:63]\n" \
  "ds_read_b64_tr_b16 v[64:65], %[ra] offset:4096\n" \
  "ds_read_b64_tr_b16 v[66:67], %[ra] offset:4608\n" \
  "ds_read_b64_tr_b16 v[68:69], %[rb] offset:4096\n" \
  "ds_read_b64_tr_b16 v[70:71], %[rb] offset:4608\n" \
  "s_waitcnt lgkmcnt(8)\n" \
  "v_mfma_f32_32x32x16_bf16 v[0:15], v[144:147], v[128:131], v[0:15]\n" \
  "v_mfma_f32_32x32x16_bf16 v[16:31], v[144:147], v[132:135], v[16:31]\n" \
  "v_mfma_f32_32x32x16_bf16 v[32:47], v[148:151], v[128:131], v[32:47]\n" \
  "v_mfma_f32_32x32x16_bf16 v[48:63], v[148:151], v[132:135], v[48:63]\n" \
  "ds_read_b64_tr_b16 v[144:145], %[ra] offset:0\n" \
  "ds_read_b64_tr_b16 v[146:147], %[ra] offset:512\n" \
  "ds_read_b64_tr_b16 v[148:149], %[rb] offset:0\n" \
  "ds_read_b64_tr_b16 v[150:151], %[rb] offset:512\n" \
  "s_waitcnt lgkmcnt(8)\n" \
  "v_mfma_f32_32x32x16_bf16 v[0:15], v[152:155], v[136:139], v[0:15]\n" \
  "v_mfma_f32_32x32x16_bf16 v[16:31], v[152:155], v[140:143], v[16:31]\n" \
  "v_mfma_f32_32x32x16_bf16 v[32:47], v[156:159], v[136:139], v[32:47]\n" \
  "v_mfma_f32_32x32x16_bf16 v[48:63], v[156:159], v[140:143], v[48:63]\n" \
  "ds_read_b64_tr_b16 v[152:153], %[ra] offset:2048\n" \
  "ds_read_b64_tr_b16 v[154:155], %[ra] offset:2560\n" \
  "ds_read_b64_tr_b16 v[156:157], %[rb] offset:2048\n" \
  "ds_read_b64_tr_b16 v[158:159], %[rb] offset:2560\n" \
  "s_waitcnt lgkmcnt(8)\n" \
  "v_mfma_f32_32x32x16_bf16 v[0:15], v[64:67], v[72:75], v[0:15]\n" \
  "v_mfma_f32_32x32x16_bf16 v[16:31], v[64:67], v[76:79], v[16:31]\n" \
  "v_mfma_f32_32x32x16_bf16 v[32:47], v[68:71], v[72:75], v[32:47]\n" \
  "v_mfma_f32_32x32x16_bf16 v[48:63], v[68:71], v[76:79], v[48:63]\n" \
  "s_waitcnt lgkmcnt(4)\n" \
  "v_mfma_f32_32x32x16_bf16 v[0:15], v[144:147], v[80:83], v[0:15]\n" \
  "v_mfma_f32_32x32x16_bf16 v[16:31], v[144:147], v[84:87], v[16:31]\n" \
  "v_mfma_f32_32x32x16_bf16 v[32:47], v[148:151], v[80:83], v[32:47]\n" \
  "v_mfma_f32_32x32x16_bf16 v[48:63], v[148:151], v[84:87], v[48:63]\n" \
  "s_waitcnt lgkmcnt(0)\n" \
  "v_mfma_f32_32x32x16_bf16 v[0:15], v[152:155], v[88:91], v[0:15]\n" \
  "v_mfma_f32_32x32x16_bf16 v[16:31], v[152:155], v[92:95], v[16:31]\n" \
  "v_mfma_f32_32x32x16_bf16 v[32:47], v[156:159], v[88:91], v[32:47]\n" \
  "v_mfma_f32_32x32x16_bf16 v[48:63], v[156:159], v[92:95], v[48:63]\n" \
  "s_nop 15\n"
#define FFN_CLOB "v64", "v65", "v66", "v67", "v68", "v69", "v70", "v71", "v72", "v73", "v74", "v75", "v76", "v77", "v78", "v79", "v80", "v81", "v82", "v83", "v84", "v85", "v86", "v87", "v88", "v89", "v90", "v91", "v92", "v93", "v94", "v95", "v100", "v101", "v102", "v103", "v104", "v105", "v106", "v107", "v108", "v109", "v110", "v111", "v112", "v113", "v114", "v115", "v116", "v117", "v118", "v119", "v120", "v121", "v122", "v123", "v124", "v125", "v126", "v127", "v128", "v129", "v130", "v131", "v132", "v133", "v134", "v135", "v136", "v137", "v138", "v139", "v140", "v141", "v142", "v143", "v144", "v145", "v146", "v147", "v148", "v149", "v150", "v151", "v152", "v153", "v154", "v155", "v156", "v157", "v158", "v159", "s36", "s37", "s38", "s39", "s40", "s41", "s42", "s43", "s44", "s45", "s46", "s47", "s48"

#define TAIL_ASM \
  "ds_read_b64_tr_b16 v[96:97], %[la] offset:0\n" \
  "ds_read_b64_tr_b16 v[98:99], %[la] offset:1024\n" \
  "ds_read_b64_tr_b16 v[100:101], %[lb] offset:0\n" \
  "ds_read_b64_tr_b16 v[102:103], %[lb] offset:1024\n" \
  "ds_read_b64_tr_b16 v[104:105], %[la] offset:2048\n" \
  "ds_read_b64_tr_b16 v[106:107], %[la] offset:3072\n" \
  "ds_read_b64_tr_b16 v[108:109], %[lb] offset:2048\n" \
  "ds_read_b64_tr_b16 v[110:111], %[lb] offset:3072\n" \
  "s_waitcnt lgkmcnt(4)\n" \
  "ds_read_b64_tr_b16 v[112:113], %[la] offset:4096\n" \
  "ds_read_b64_tr_b16 v[114:115], %[la] offset:5120\n" \
  "ds_read_b64_tr_b16 v[116:117], %[lb] offset:4096\n" \
  "ds_read_b64_tr_b16 v[118:119], %[lb] offset:5120\n" \
  "ds_read_b64_tr_b16 v[120:121], %[la] offset:6144\n" \
  "ds_read_b64_tr_b16 v[122:123], %[la] offset:7168\n" \
  "ds_read_b64_tr_b16 v[124:125], %[lb] offset:6144\n" \
  "ds_read_b64_tr_b16 v[126:127], %[lb] offset:7168\n" \
  "s_waitcnt lgkmcnt(4)\n" \
  "ds_read_b64_tr_b16 v[128:129], %[la] offset:8192\n" \
  "ds_read_b64_tr_b16 v[130:131], %[la] offset:9216\n" \
  "ds_read_b64_tr_b16 v[132:133], %[lb] offset:8192\n" \
  "ds_read_b64_tr_b16 v[134:135], %[lb] offset:9216\n" \
  "ds_read_b64_tr_b16 v[136:137], %[la] offset:10240\n" \
  "ds_read_b64_tr_b16 v[138:139], %[la] offset:11264\n" \
  "ds_read_b64_tr_b16 v[140:141], %[lb] offset:10240\n" \
  "ds_read_b64_tr_b16 v[142:143], %[lb] offset:11264\n" \
  "s_waitcnt lgkmcnt(4)\n" \
  "ds_read_b64_tr_b16 v[144:145], %[la] offset:12288\n" \
  "ds_read_b64_tr_b16 v[146:147], %[la] offset:13312\n" \
  "ds_read_b64_tr_b16 v[148:149], %[lb] offset:12288\n" \
  "ds_read_b64_tr_b16 v[150:151], %[lb] offset:13312\n" \
  "ds_read_b64_tr_b16 v[152:153], %[la] offset:14336\n" \
  "ds_read_b64_tr_b16 v[154:155], %[la] offset:15360\n" \
  "ds_read_b64_tr_b16 v[156:157], %[lb] offset:14336\n" \
  "ds_read_b64_tr_b16 v[158:159], %[lb] offset:15360\n" \
  "s_waitcnt lgkmcnt(4)\n" \
  "v_add_u32 v162, 0x1000, %[vo]\n" \
  "v_mov_b32 v163, 1\n" \
  "s_mov_b64 exec, 1\n" \
  "ds_add_u32 %[pc], v163\n" \
  "s_mov_b64 exec, -1\n" \
  "s_mov_b32 %[todo], 0xff\n" \
  "s_mov_b32 %[nx], 0\n" \
  "s_mov_b32 %[sp], 0x4000\n" \
  "s_sleep 4\n" \
  "Lt_loop%=:\n" \
  "s_bitcmp1_b32 %[todo], 0\n" \
  "s_cbranch_scc0 Lt_s0_%=\n" \
  "global_load_dwordx4 v[32:35], %[vo], %[ob] offset:0 sc1\n" \
  "Lt_s0_%=:\n" \
  "s_bitcmp1_b32 %[todo], 1\n" \
  "s_cbranch_scc0 Lt_s1_%=\n" \
  "global_load_dwordx4 v[36:39], %[vo], %[ob] offset:1024 sc1\n" \
  "Lt_s1_%=:\n" \
  "s_bitcmp1_b32 %[todo], 2\n" \
  "s_cbranch_scc0 Lt_s2_%=\n" \
  "global_load_dwordx4 v[40:43], %[vo], %[ob] offset:2048 sc1\n" \
  "Lt_s2_%=:\n" \
  "s_bitcmp1_b32 %[todo], 3\n" \
  "s_cbranch_scc0 Lt_s3_%=\n" \
  "global_load_dwordx4 v[44:47], %[vo], %[ob] offset:3072 sc1\n" \
  "Lt_s3_%=:\n" \
  "s_bitcmp1_b32 %[todo], 4\n" \
  "s_cbranch_scc0 Lt_s4_%=\n" \
  "global_load_dwordx4 v[48:51], v162, %[ob] offset:0 sc1\n" \
  "Lt_s4_%=:\n" \
  "s_bitcmp1_b32 %[todo], 5\n" \
  "s_cbranch_scc0 Lt_s5_%=\n" \
  "global_load_dwordx4 v[52:55], v162, %[ob] offset:1024 sc1\n" \
  "Lt_s5_%=:\n" \
  "s_bitcmp1_b32 %[todo], 6\n" \
  "s_cbranch_scc0 Lt_s6_%=\n" \
  "global_load_dwordx4 v[56:59], v162, %[ob] offset:2048 sc1\n" \
  "Lt_s6_%=:\n" \
  "s_bitcmp1_b32 %[todo], 7\n" \
  "s_cbranch_scc0 Lt_s7_%=\n" \
  "global_load_dwordx4 v[60:63], v162, %[ob] offset:3072 sc1\n" \
  "Lt_s7_%=:\n" \
  "s_getpc_b64 s[52:53]\n" \
  "Lt_pc%=:\n" \
  "s_mul_i32 %[t], %[nx], 24\n" \
  "s_add_u32 %[t], %[t], Lt_chain%=-Lt_pc%=\n" \
  "s_add_u32 s52, s52, %[t]\n" \
  "s_addc_u32 s53, s53, 0\n" \
  "s_waitcnt vmcnt(0)\n" \
  "ds_read_b32 v164, %[pc]\n" \
  "v_max3_u32 v160, v32, v33, v34\n" \
  "v_max_u32 v160, v160, v35\n" \
  "v_cmp_eq_u32 s[36:37], -1, v160\n" \
  "v_max3_u32 v161, v36, v37, v38\n" \
  "v_max_u32 v161, v161, v39\n" \
  "v_cmp_eq_u32 s[38:39], -1, v161\n" \
  "v_max3_u32 v160, v40, v41, v42\n" \
  "v_max_u32 v160, v160, v43\n" \
  "v_cmp_eq_u32 s[40:41], -1, v160\n" \
  "v_max3_u32 v161, v44, v45, v46\n" \
  "v_max_u32 v161, v161, v47\n" \
  "v_cmp_eq_u32 s[42:43], -1, v161\n" \
  "v_max3_u32 v160, v48, v49, v50\n" \
  "v_max_u32 v160, v160, v51\n" \
  "v_cmp_eq_u32 s[44:45], -1, v160\n" \
  "v_max3_u32 v161, v52, v53, v54\n" \
  "v_max_u32 v161, v161, v55\n" \
  "v_cmp_eq_u32 s[46:47], -1, v161\n" \
  "v_max3_u32 v160, v56, v57, v58\n" \
  "v_max_u32 v160, v160, v59\n" \
  "v_cmp_eq_u32 s[48:49], -1, v160\n" \
  "v_max3_u32 v161, v60, v61, v62\n" \
  "v_max_u32 v161, v161, v63\n" \
  "v_cmp_eq_u32 s[50:51], -1, v161\n" \
  "s_mov_b32 %[todo], 1\n" \
  "s_cmp_lg_u64 s[50:51], 0\n" \
  "s_addc_u32 %[todo], %[todo], %[todo]\n" \
  "s_cmp_lg_u64 s[48:49], 0\n" \
  "s_addc_u32 %[todo], %[todo], %[todo]\n" \
  "s_cmp_lg_u64 s[46:47], 0\n" \
  "s_addc_u32 %[todo], %[todo], %[todo]\n" \
  "s_cmp_lg_u64 s[44:45], 0\n" \
  "s_addc_u32 %[todo], %[todo], %[todo]\n" \
  "s_cmp_lg_u64 s[42:43], 0\n" \
  "s_addc_u32 %[todo], %[todo], %[todo]\n" \
  "s_cmp_lg_u64 s[40:41], 0\n" \
  "s_addc_u32 %[todo], %[todo], %[todo]\n" \
  "s_cmp_lg_u64 s[38:39], 0\n" \
  "s_addc_u32 %[todo], %[todo], %[todo]\n" \
  "s_cmp_lg_u64 s[36:37], 0\n" \
  "s_addc_u32 %[todo], %[todo], %[todo]\n" \
  "s_ff1_i32_b32 %[nx], %[todo]\n" \
  "s_and_b32 %[todo], %[todo], 0xff\n" \
  "s_waitcnt lgkmcnt(0)\n" \
  "s_setpc_b64 s[52:53]\n" \
  "Lt_chain%=:\n" \
  "s_cmp_eq_u32 %[nx], 0\n" \
  "s_cbranch_scc1 Lt_next%=\n" \
  "v_mfma_f32_32x32x16_bf16 v[80:95], v[100:103], v[32:35], 0\n" \
  "v_mfma_f32_32x32x16_bf16 v[64:79], v[96:99], v[32:35], 0\n" \
  "s_cmp_eq_u32 %[nx], 1\n" \
  "s_cbranch_scc1 Lt_next%=\n" \
  "v_mfma_f32_32x32x16_bf16 v[80:95], v[108:111], v[36:39], v[80:95]\n" \
  "v_mfma_f32_32x32x16_bf16 v[64:79], v[104:107], v[36:39], v[64:79]\n" \
  "s_cmp_eq_u32 %[nx], 2\n" \
  "s_cbranch_scc1 Lt_next%=\n" \
  "v_mfma_f32_32x32x16_bf16 v[80:95], v[116:119], v[40:43], v[80:95]\n" \
  "v_mfma_f32_32x32x16_bf16 v[64:79], v[112:115], v[40:43], v[64:79]\n" \
  "s_cmp_eq_u32 %[nx], 3\n" \
  "s_cbranch_scc1 Lt_next%=\n" \
  "v_mfma_f32_32x32x16_bf16 v[80:95], v[124:127], v[44:47], v[80:95]\n" \
  "v_mfma_f32_32x32x16_bf16 v[64:79], v[120:123], v[44:47], v[64:79]\n" \
  "s_cmp_eq_u32 %[nx], 4\n" \
  "s_cbranch_scc1 Lt_next%=\n" \
  "v_mfma_f32_32x32x16_bf16 v[80:95], v[132:135], v[48:51], v[80:95]\n" \
  "v_mfma_f32_32x32x16_bf16 v[64:79], v[128:131], v[48:51], v[64:79]\n" \
  "s_cmp_eq_u32 %[nx], 5\n" \
  "s_cbranch_scc1 Lt_next%=\n" \
  "v_mfma_f32_32x32x16_bf16 v[80:95], v[140:143], v[52:55], v[80:95]\n" \
  "v_mfma_f32_32x32x16_bf16 v[64:79], v[136:139], v[52:55], v[64:79]\n" \
  "s_cmp_eq_u32 %[nx], 6\n" \
  "s_cbranch_scc1 Lt_next%=\n" \
  "v_mfma_f32_32x32x16_bf16 v[80:95], v[148:151], v[56:59], v[80:95]\n" \
  "v_mfma_f32_32x32x16_bf16 v[64:79], v[144:147], v[56:59], v[64:79]\n" \
  "s_cmp_eq_u32 %[nx], 7\n" \
  "s_cbranch_scc1 Lt_next%=\n" \
  "v_mfma_f32_32x32x16_bf16 v[80:95], v[156:159], v[60:63], v[80:95]\n" \
  "v_mfma_f32_32x32x16_bf16 v[64:79], v[152:155], v[60:63], v[64:79]\n" \
  "s_branch Lt_done%=\n" \
  "Lt_next%=:\n" \
  "s_sub_u32 %[sp], %[sp], 1\n" \
  "s_cmp_lg_u32 %[sp], 0\n" \
  "s_cbranch_scc1 Lt_loop%=\n" \
  "Lt_done%=:\n" \
  "s_mov_b32 %[sp], 0x40000\n" \
  "Lt_pre%=:\n" \
  "v_readfirstlane_b32 %[t], v164\n" \
  "s_cmp_ge_u32 %[t], 2\n" \
  "s_cbranch_scc1 Lt_prex%=\n" \
  "s_sleep 1\n" \
  "ds_read_b32 v164, %[pc]\n" \
  "s_waitcnt lgkmcnt(0)\n" \
  "s_sub_u32 %[sp], %[sp], 1\n" \
  "s_cmp_lg_u32 %[sp], 0\n" \
  "s_cbranch_scc1 Lt_pre%=\n" \
  "Lt_prex%=:\n" \
  "s_nop 7\n" \
  "ds_write_b128 %[pw], v[80:83] offset:4096\n" \
  "ds_write_b128 %[pw], v[84:87] offset:5120\n" \
  "ds_write_b128 %[pw], v[88:91] offset:6144\n" \
  "ds_write_b128 %[pw], v[92:95] offset:7168\n" \
  "ds_write_b128 %[pw], v[72:75] offset:2048\n" \
  "ds_write_b128 %[pw], v[76:79] offset:3072\n" \
  "s_mov_b64 exec, 1\n" \
  "ds_add_u32 %[fc], v163\n" \
  "s_mov_b64 exec, -1\n" \
  "s_mov_b32 %[sp], 0x40000\n" \
  "Lt_fin%=:\n" \
  "ds_read_b32 v164, %[fc]\n" \
  "ds_read_b128 v[96:99], %[pa] offset:0\n" \
  "ds_read_b128 v[100:103], %[pa] offset:1024\n" \
  "ds_read_b128 v[104:107], %[pa] offset:4096\n" \
  "ds_read_b128 v[108:111], %[pa] offset:5120\n" \
  "s_waitcnt lgkmcnt(10)\n" \
  "ds_read_b128 v[112:115], %[pa] offset:8192\n" \
  "ds_read_b128 v[116:119], %[pa] offset:9216\n" \
  "ds_read_b128 v[120:123], %[pa] offset:12288\n" \
  "ds_read_b128 v[124:127], %[pa] offset:13312\n" \
  "s_waitcnt lgkmcnt(8)\n" \
  "ds_read_b128 v[128:131], %[p1] offset:2048\n" \
  "ds_read_b128 v[132:135], %[p1] offset:3072\n" \
  "ds_read_b128 v[136:139], %[p2] offset:4096\n" \
  "ds_read_b128 v[140:143], %[p2] offset:5120\n" \
  "ds_read_b128 v[144:147], %[p3] offset:6144\n" \
  "ds_read_b128 v[148:151], %[p3] offset:7168\n" \
  "v_readfirstlane_b32 %[t], v164\n" \
  "s_cmp_ge_u32 %[t], 8\n" \
  "s_cbranch_scc1 Lt_finx%=\n" \
  "Lt_fw%=:\n" \
  "s_sleep 1\n" \
  "ds_read_b32 v164, %[fc]\n" \
  "s_waitcnt lgkmcnt(0)\n" \
  "v_readfirstlane_b32 %[t], v164\n" \
  "s_cmp_ge_u32 %[t], 8\n" \
  "s_cbranch_scc1 Lt_fin%=\n" \
  "s_sub_u32 %[sp], %[sp], 1\n" \
  "s_cmp_lg_u32 %[sp], 0\n" \
  "s_cbranch_scc1 Lt_fw%=\n" \
  "Lt_finx%=:\n" \
  "s_waitcnt lgkmcnt(6)\n" \
  "v_pk_add_f32 v[96:97], v[96:97], v[104:105]\n" \
  "v_pk_add_f32 v[112:113], v[112:113], v[120:121]\n" \
  "v_pk_add_f32 v[98:99], v[98:99], v[106:107]\n" \
  "v_pk_add_f32 v[114:115], v[114:115], v[122:123]\n" \
  "v_pk_add_f32 v[100:101], v[100:101], v[108:109]\n" \
  "v_pk_add_f32 v[116:117], v[116:117], v[124:125]\n" \
  "v_pk_add_f32 v[102:103], v[102:103], v[110:111]\n" \
  "v_pk_add_f32 v[118:119], v[118:119], v[126:127]\n" \
  "v_pk_add_f32 v[96:97], v[96:97], v[112:113]\n" \
  "v_pk_add_f32 v[98:99], v[98:99], v[114:115]\n" \
  "v_pk_add_f32 v[100:101], v[100:101], v[116:117]\n" \
  "v_pk_add_f32 v[102:103], v[102:103], v[118:119]\n" \
  "v_fma_f32 v64, v96, %[og], v64\n" \
  "v_fma_f32 v65, v97, %[og], v65\n" \
  "v_fma_f32 v66, v98, %[og], v66\n" \
  "v_fma_f32 v67, v99, %[og], v67\n" \
  "v_fma_f32 v68, v100, %[og], v68\n" \
  "v_fma_f32 v69, v101, %[og], v69\n" \
  "v_fma_f32 v70, v102, %[og], v70\n" \
  "v_fma_f32 v71, v103, %[og], v71\n" \
  "s_waitcnt lgkmcnt(0)\n" \
  "v_pk_add_f32 v[64:65], v[64:65], v[128:129]\n" \
  "v_pk_add_f32 v[136:137], v[136:137], v[144:145]\n" \
  "v_pk_add_f32 v[66:67], v[66:67], v[130:131]\n" \
  "v_pk_add_f32 v[138:139], v[138:139], v[146:147]\n" \
  "v_pk_add_f32 v[68:69], v[68:69], v[132:133]\n" \
  "v_pk_add_f32 v[140:141], v[140:141], v[148:149]\n" \
  "v_pk_add_f32 v[70:71], v[70:71], v[134:135]\n" \
  "v_pk_add_f32 v[142:143], v[142:143], v[150:151]\n" \
  "v_pk_add_f32 v[64:65], v[64:65], v[136:137]\n" \
  "v_pk_add_f32 v[66:67], v[66:67], v[138:139]\n" \
  "v_pk_add_f32 v[68:69], v[68:69], v[140:141]\n" \
  "v_pk_add_f32 v[70:71], v[70:71], v[142:143]\n" \
  "v_cvt_pk_bf16_f32 v96, v64, v65\n" \
  "v_cvt_pk_bf16_f32 v97, v66, v67\n" \
  "v_cvt_pk_bf16_f32 v98, v68, v69\n" \
  "v_cvt_pk_bf16_f32 v99, v70, v71\n" \
  "s_nop 1\n" \
  "v_permlane32_swap_b32 v96, v98\n" \
  "v_permlane32_swap_b32 v97, v99\n" \
  "s_nop 1\n" \
  "global_store_dwordx4 %[oo], v[96:99], %[out]\n" \
  "s_nop 1\n"
#define TAIL_CLOB "v32", "v33", "v34", "v35", "v36", "v37", "v38", "v39", "v40", "v41", "v42", "v43", "v44", "v45", "v46", "v47", "v48", "v49", "v50", "v51", "v52", "v53", "v54", "v55", "v56", "v57", "v58", "v59", "v60", "v61", "v62", "v63", "v64", "v65", "v66", "v67", "v68", "v69", "v70", "v71", "v72", "v73", "v74", "v75", "v76", "v77", "v78", "v79", "v80", "v81", "v82", "v83", "v84", "v85", "v86", "v87", "v88", "v89", "v90", "v91", "v92", "v93", "v94", "v95", "v96", "v97", "v98", "v99", "v100", "v101", "v102", "v103", "v104", "v105", "v106", "v107", "v108", "v109", "v110", "v111", "v112", "v113", "v114", "v115", "v116", "v117", "v118", "v119", "v120", "v121", "v122", "v123", "v124", "v125", "v126", "v127", "v128", "v129", "v130", "v131", "v132", "v133", "v134", "v135", "v136", "v137", "v138", "v139", "v140", "v141", "v142", "v143", "v144", "v145", "v146", "v147", "v148", "v149", "v150", "v151", "v152", "v153", "v154", "v155", "v156", "v157", "v158", "v159", "v160", "v161", "v162", "v163", "v164", "s36", "s37", "s38", "s39", "s40", "s41", "s42", "s43", "s44", "s45", "s46", "s47", "s48", "s49", "s50", "s51", "s52", "s53"

__global__ __launch_bounds__(768, 3) void fused_k(const char* __restrict__ x, const char* __restrict__ wqkv,
                                                  const char* __restrict__ wo, const char* __restrict__ w1,
                                                  const char* __restrict__ b1, const char* __restrict__ w2,
                                                  const char* __restrict__ b2, const char* __restrict__ gw,
                                                  char* ws, char* __restrict__ out) {
  extern __shared__ __attribute__((aligned(16))) char smem[];
  const int tid = threadIdx.x;
  const int wid = __builtin_amdgcn_readfirstlane(tid >> 6);
  const int xg = blockIdx.x & 7, ig = blockIdx.x >> 3;
  const int qb = (xg & 1) * 4 + (ig & 3), h = (xg >> 1) * 8 + (ig >> 2);
  const bool ffn = (h & 7) < 7;
  const int hf = (h >> 3) * 7 + (ffn ? h & 7 : 6);
  const unsigned lds0 = __builtin_amdgcn_readfirstlane((unsigned)(uintptr_t)smem);
  lds_ptr L = (lds_ptr)smem;
  const __amdgpu_buffer_rsrc_t rs = __builtin_amdgcn_make_buffer_rsrc(ws, 0, WS_END, 0x00020000);
  const int u = h >> 3, ct = h & 7;
  const lds_cptr cnt = (lds_cptr)L + L_CNT;
  asm volatile("" :: "s"(x), "s"(wqkv), "s"(w1), "s"(b1), "s"(gw), "s"(ws));
  if (tid < 32) *(LAS unsigned*)(L + L_CNT + tid * 4) = tid == C_READY + 8 ? 8u : 0u;

  const int j = wid & 3;
  const int rot = (h + qb) & 1;
#define RC(c) ((c) ^ rot)
  const char* xs = x + (size_t)(qb * 256 + j * 64) * 1024;
  const char* wq = wqkv + h * 32 + (size_t)j * 16 * 3072;
  const char* ww = w1 + hf * 128 + (size_t)j * 16 * (FF * 2);
#define DMA_X(c, sb, i0) { _Pragma("unroll") for (int i = (i0); i < (i0) + 4; ++i)                              \
      glds16(xs + RC(c) * 128 + i * 8192, (i & 1) ? xv1 : xv0, (sb) + (j * 8 + i) * 1024); }
#define DMA_QKV(c, sb) { glds16(wq + (size_t)RC(c) * 64 * 3072, qv_, (sb) + ST_QK + j * 1024);                    \
      if (lane < 32) glds16(wq + (size_t)RC(c) * 64 * 3072, vv_, (sb) + ST_V + j * 512); }
#define DMA_W1(c, sb) { glds16(ww + (size_t)RC(c) * 64 * (FF * 2), wv0, (sb) + ST_W1 + j * 2048);                 \
      glds16(ww + (size_t)(RC(c) * 64 + 8) * (FF * 2), wv1, (sb) + ST_W1 + j * 2048 + 1024); }

  if (wid < 8) {
    LANE_VARS DMA_VARS
    bf16x8 qf;
    int tile;
    if (wid >= 4) {
      __builtin_amdgcn_s_setprio(3);
      __builtin_amdgcn_s_sleep(6);
      unsigned so = 2 * ST_BYTES;
#pragma unroll 1
      for (int c = 1; c < 8; ++c) {
        const unsigned sb = lds0 + so;
        DMA_QKV(c, sb) DMA_X(c, sb, 0)
        asm volatile("s_waitcnt vmcnt(6)\n\ts_barrier" ::: "memory");
        DMA_X(c, sb, 4)
        if (ffn) DMA_W1(c, sb)
        so = so == 2 * ST_BYTES ? 0 : so + ST_BYTES;
      }
      asm volatile("s_waitcnt vmcnt(0)\n\ts_barrier" ::: "memory");
      tile = qb * 8 + 2 * j + 1;
    } else {
      DMA_QKV(0, lds0 + ST_BYTES) DMA_X(0, lds0 + ST_BYTES, 0)
      __builtin_amdgcn_s_setprio(2);
      if (wid == 0 && !ffn) *(LAS u32x4*)(L + L_GATE + lane * 16) = *(const u32x4*)(gw + (lane ^ (rot * 8)) * 16);
      if (wid == 1 && lane == 0) *(LAS u64*)(L + L_E) = __hip_atomic_load((gu64*)(ws + WS_E), RLX_AGENT);
      const unsigned xo0 = (wid * 64 + r16) * 128 + ((g4 ^ (r16 >> 1)) << 4), xo1 = (wid * 64 + r16) * 128 + (((4 + g4) ^ (r16 >> 1)) << 4);
      const unsigned qo = ST_QK + (8 * g4 + q4) * 64 + g1 * 32 + p4 * 8, ko = qo ^ 32;
      const unsigned vo0 = ST_V + (8 * g4 + 4 * g1 + q4) * 32 + p4 * 8, vo1 = vo0 ^ 128;
      f32x4 aq[4], ak[4], av[4];
#pragma unroll
      for (int m = 0; m < 4; ++m) { aq[m] = f32x4{}; ak[m] = f32x4{}; av[m] = f32x4{}; PIN(aq[m]); PIN(ak[m]); PIN(av[m]); }
      bf16x8 fx[2][4], fq[2], fk[2], fv[2];
#define LOAD_A(b, SB) { const lds_cptr B_ = (lds_cptr)L + (SB);                                                \
        fq[b] = ldtr2(B_ + qo + (b) * 2048, B_ + qo + (b) * 2048 + 256);                                       \
        _Pragma("unroll") for (int m = 0; m < 4; ++m) fx[b][m] = ld128(B_ + ((b) ? xo1 : xo0) + m * 2048);     \
        fk[b] = ldtr2(B_ + ko + (b) * 2048, B_ + ko + (b) * 2048 + 256);                                       \
        fv[b] = ldtr2(B_ + vo0 + (b) * 1024, B_ + vo1 + (b) * 1024); }
#define MMA_A(b) { _Pragma("unroll") for (int m = 0; m < 4; ++m) aq[m] = MFMA16(fq[b], fx[b][m], aq[m]);       \
        _Pragma("unroll") for (int m = 0; m < 4; ++m) ak[m] = MFMA16(fk[b], fx[b][m], ak[m]);                  \
        _Pragma("unroll") for (int m = 0; m < 4; ++m) av[m] = MFMA16(fx[b][m], fv[b], av[m]); }
      asm volatile("s_waitcnt vmcnt(0) lgkmcnt(0)\n\ts_barrier" ::: "memory");
      int sb = ST_BYTES;
      LOAD_A(0, ST_BYTES)
      tile = qb * 8 + 2 * wid;
      const unsigned kvo = WS_KV + (h * 64 + tile) * 2048 + lane * 16;
#pragma unroll 1
      for (int c = 0; c < 7; ++c) {
        SBAR();
        LOAD_A(1, sb) MMA_A(0) INTERLEAVE(10)
        SBAR();
        if (c == 6) break;
        sb = sb == 2 * ST_BYTES ? 0 : sb + ST_BYTES;
        BAR();
        SBAR();
        LOAD_A(0, sb) MMA_A(1) INTERLEAVE(10)
        SBAR();
      }
      MMA_A(1)
      SBAR();
      BAR();
      LOAD_A(0, 2 * ST_BYTES) LOAD_A(1, 2 * ST_BYTES)
      SBAR();
#pragma unroll
      for (int b = 0; b < 2; ++b)
#pragma unroll
        for (int m = 0; m < 4; ++m) { ak[m] = MFMA16(fk[b], fx[b][m], ak[m]); av[m] = MFMA16(fx[b][m], fv[b], av[m]); }
#pragma unroll
      for (int b = 0; b < 2; ++b)
#pragma unroll
        for (int m = 3; m >= 0; --m) aq[m] = MFMA16(fq[b], fx[b][m], aq[m]);
      __builtin_amdgcn_s_setprio(3);
      const float qs = 0.25f * 1.4426950408889634f;
      u32x4 kv[4];
#pragma unroll
      for (int mt = 0; mt < 2; ++mt) { kv[2 * mt] = merge16(ak[2 * mt], ak[2 * mt + 1], 1.0f); kv[2 * mt + 1] = merge16(av[2 * mt], av[2 * mt + 1], 1.0f); }
#pragma unroll
      for (int i = 0; i < 4; ++i) st_sc1(kv[i], rs, kvo + i * 1024);
#pragma unroll
      for (int i = 0; i < 4; ++i) *(LAS u32x4*)(L + wid * 4096 + i * 1024 + lane * 16) = kv[i];
      *(LAS u32x4*)(L + L_QX + wid * 1024 + lane * 16) = merge16(aq[2], aq[3], qs);
      if (lane < 2) asm volatile("ds_add_u32 %0, %1" :: "v"(lds0 + L_CNT + (lane ? C_DONE : C_READY) * 4), "v"(2u - lane) : "memory");
      qf = __builtin_bit_cast(bf16x8, merge16(aq[0], aq[1], qs));
    }
    const lds_cptr kp = (lds_cptr)L + lane * 16;
    const unsigned one = (r32 == 16 || r32 == 20) ? 0x3f803f80u : 0u;
    f32x16 negm, o = f32x16{}, sa, sb = f32x16{};
    bf16x8 v0 = __builtin_bit_cast(bf16x8, u32x4{one, one, one, one}), v1 = v0, v2 = v0, v3 = v0;
    unsigned ka = lds0 + lane * 16, va_ = lds0 + 1024 + hh * 512 + r16 * 16, ca = lds0 + L_CNT + C_READY * 4, ix = 0xdddd;
    asm volatile("" : "+{v[112:127]}"(o), "+{v[80:95]}"(sb), "+{v[136:139]}"(v0), "+{v[140:143]}"(v1), "+{v[144:147]}"(v2), "+{v[148:151]}"(v3),
                 "+{v152}"(ka), "+{v153}"(va_), "+{v154}"(ca), "+{v156}"(ix));
#define LOAD_V() asm volatile("s_mov_b64 exec, %3\n\tds_read_b128 %0, %2\n\tds_read_b128 %1, %2 offset:256\n\ts_mov_b64 exec, -1\n\ts_waitcnt lgkmcnt(0)" \
                              : "+{v[144:147]}"(v2), "+{v[148:151]}"(v3) : "{v153}"(va_), "s"(0x0000ffff0000ffffull) : "memory")
    if (wid < 4) flag_set(ws, WS_FKV + ((h * 4 + wid) * 8 + qb) * 128, make_tag(*(const LAS u64*)(L + L_E)), lane);
    lds_wait(cnt + C_READY * 4, 8);
    if (wid >= 4) qf = ld128((lds_cptr)L + L_QX + j * 1024 + lane * 16);
    bf16x8 kf = ld128(kp + 2048);
    {
      const bf16x8 k0 = ld128(kp);
      LOAD_V();
      const f32x16 s0 = MFMA(k0, qf, f32x16{});
      const float m = swap_max(max16(s0));
#pragma unroll
      for (int r = 0; r < 16; ++r) negm[r] = -m;
      sa = MFMA(k0, qf, negm);
    }
    float g = 0.f;
#pragma unroll 1
    for (int seg = 0; seg < 4; ++seg) {
      unsigned n_ = seg == 1 ? 4 : (seg == 2 ? 2 : 1), t_, sp_;
      if (seg < 2) __builtin_amdgcn_s_setprio(2); else if (seg == 2 || wid >= 4) __builtin_amdgcn_s_setprio(1); else __builtin_amdgcn_s_setprio(0);
      asm volatile(ATT_ASM
        : "+{v[64:79]}"(sa), "+{v[80:95]}"(sb), "+{v[112:127]}"(o), "+{v[132:135]}"(kf), "+{v[136:139]}"(v0), "+{v[140:143]}"(v1),
          "+{v[144:147]}"(v2), "+{v[148:151]}"(v3), "+{v152}"(ka), "+{v153}"(va_), "+{v154}"(ca), [n] "+s"(n_), [tmp] "=&s"(t_), [spin] "=&s"(sp_)
        : "{v[96:111]}"(negm), "{v[128:131]}"(qf), "{v156}"(ix), [msk] "s"(0x0000ffff0000ffffull)
        : "v155", "scc", "memory");
      if (seg == 0) {
        lds_wait(cnt + (C_READY + 1) * 4, 8);
        sa = MFMA(ld128(kp + 16384), qf, negm);
        kf = ld128(kp + 16384 + 2048);
        LOAD_V();
        ca = lds0 + L_CNT + (C_READY + 2) * 4;
      } else if (seg == 1) {
        lds_arrive(lds0 + L_CNT + C_HALF * 4, 1, lane);
      } else if (seg == 2) {
        g = *(const LAS float*)(L + L_G + ((tile & 7) * 32 + r32) * 4);
      }
    }
    lds_arrive(lds0 + L_CNT + C_END * 4, 1, lane);
    __builtin_amdgcn_s_setprio(3);
    o = pv(v1, sb, 1, o);
    float chk = o[8] * 134217728.0f;
#pragma unroll
    for (int r = 0; r < 8; ++r) chk += fabsf(o[r]);
    const bool ok = chk < 1.7e38f;
    if (__builtin_expect(!__all(ok), 0)) {
      flags_wait(ws, WS_FKV + (h * 32 + r32) * 128, make_tag(*(const LAS u64*)(L + L_E)));
      float m = -INFINITY;
      o = f32x16{};
#pragma unroll 1
      for (int t = 0; t < 64; ++t) {
        const unsigned tb = WS_KV + (h * 64 + t) * 2048;
        const u32x4 kk = ld_sc1(rs, tb + lane * 16, 0);
        u32x4 va = ld_sc1(rs, tb + 1024 + hh * 512 + r16 * 16, 0), vb = ld_sc1(rs, tb + 1280 + hh * 512 + r16 * 16, 0);
        if (g1) { va = u32x4{one, one, one, one}; vb = va; }
        f32x16 z = MFMA(__builtin_bit_cast(bf16x8, kk), qf, f32x16{});
        const float mn = fmaxf(m, swap_max(max16(z)));
        const float al = __builtin_amdgcn_exp2f(m - mn);
        m = mn;
#pragma unroll
        for (int r = 0; r < 16; ++r) { z[r] = __builtin_amdgcn_exp2f(z[r] - m); o[r] *= al; }
        o = pv(__builtin_bit_cast(bf16x8, va), z, 0, o);
        o = pv(__builtin_bit_cast(bf16x8, vb), z, 1, o);
      }
    }
    const float sc = g * __builtin_amdgcn_rcpf(o[8]);
    u32x4 ov;
#pragma unroll
    for (int i = 0; i < 4; ++i) ov[i] = cvtpk(o[2 * i] * sc, o[2 * i + 1] * sc);
    st_sc1(not_empty(ov), rs, WS_A + (tile * KSTEPS + h) * 1024 + lane * 16);
    const int mt = wid & 1, kq = wid >> 1;
    __builtin_amdgcn_s_setprio(3);
    {
      LANE_VARS
      const float og = bfround(1.0f - *(const LAS float*)(L + L_G + (u * 64 + mt * 32 + r32) * 4));
      lds_wait(cnt + C_WO * 4, 4);
      const unsigned la = lds0 + kq * 16384 + (4 * hh + q4) * 128 + ((g1 ^ (kq & 1)) * 32) + p4 * 8 + (((q4 >> 1) ^ (kq >> 1)) * 64);
      const unsigned pw = lds0 + kq * 16384 + mt * 8192 + lane * 16;
      unsigned todo, nx, sp, t;
      asm volatile(TAIL_ASM
        : [todo] "=&s"(todo), [nx] "=&s"(nx), [sp] "=&s"(sp), [t] "=&s"(t)
        : [la] "v"(la), [lb] "v"(la ^ 64), [vo] "v"(lane * 16), [pc] "v"(lds0 + L_CNT + (C_PRE + kq) * 4), [fc] "v"(lds0 + L_CNT + (C_FIN + mt) * 4),
          [pw] "v"(pw), [p1] "v"(pw ^ 16384), [p2] "v"(pw ^ 32768), [p3] "v"(pw ^ 49152),
          [pa] "v"(lds0 + 65536 + ((kq >> 1) * 2 + mt) * 16384 + (kq & 1) * 2048 + lane * 16), [og] "v"(og),
          [oo] "v"((qb * 256 + u * 64 + mt * 32 + r32) * 1024 + (ct * 64 + kq * 16 + 8 * hh) * 2), [out] "s"(out),
          [ob] "s"(ws + WS_A + (size_t)((qb * 8 + u * 2 + mt) * KSTEPS + kq * 8) * 1024)
        : TAIL_CLOB, "scc", "memory");
      if (blockIdx.x == 0 && wid == 0 && lane == 0) __hip_atomic_store((gu64*)(ws + WS_E), make_tag(*(const LAS u64*)(L + L_E)), RLX_AGENT);
    }
  } else {
    const int v = wid - 8;
    LANE_VARS DMA_VARS
    DMA_W1(0, lds0 + ST_BYTES) DMA_X(0, lds0 + ST_BYTES, 4)
    f32x4 ah[4][4];
    {
#pragma unroll
      for (int i = 0; i < 4; ++i) {
        const u32x2 bw = *(const u32x2*)(b1 + (hf * 64 + i * 16 + 4 * g4) * 2);
        ah[i][0] = f32x4{__uint_as_float(bw[0] << 16), __uint_as_float(bw[0] & 0xffff0000u), __uint_as_float(bw[1] << 16), __uint_as_float(bw[1] & 0xffff0000u)};
        ah[i][1] = ah[i][0]; ah[i][2] = ah[i][0]; ah[i][3] = ah[i][0];
      }
      const unsigned xo0 = (v * 64 + r16) * 128 + ((g4 ^ (r16 >> 1)) << 4), xo1 = (v * 64 + r16) * 128 + (((4 + g4) ^ (r16 >> 1)) << 4);
      const unsigned wo0 = ST_W1 + (8 * g4 + q4) * 128 + (((q4 >> 1) | (2 * g1)) * 32) + p4 * 8;
      bf16x8 fx[2][4], fw[2][4];
#define LOAD_F(b, SB) { const lds_cptr B_ = (lds_cptr)L + (SB);                                                \
        _Pragma("unroll") for (int m = 0; m < 4; ++m) {                                                        \
          fw[b][m] = ldtr2(B_ + (wo0 ^ (m * 32)) + (b) * 4096, B_ + (wo0 ^ (m * 32)) + (b) * 4096 + 512);      \
          fx[b][m] = ld128(B_ + ((b) ? xo1 : xo0) + m * 2048); } }
#define MMA_F(b) { _Pragma("unroll") for (int i = 0; i < 4; ++i) _Pragma("unroll") for (int m = 0; m < 4; ++m) \
          ah[i][m] = MFMA16(fw[b][i], fx[b][m], ah[i][m]); }
#pragma unroll
      for (int i = 0; i < 4; ++i) { PIN(ah[i][0]); PIN(ah[i][1]); PIN(ah[i][2]); PIN(ah[i][3]); }
      asm volatile("s_waitcnt vmcnt(0) lgkmcnt(0)\n\ts_barrier" ::: "memory");
      if (ffn) {
        int sb = ST_BYTES;
      LOAD_F(0, ST_BYTES)
#pragma unroll 1
      for (int c = 0; c < 8; ++c) {
        SBAR();
        LOAD_F(1, sb) MMA_F(0) INTERLEAVE(12)
        SBAR();
        sb = sb == 2 * ST_BYTES ? 0 : sb + ST_BYTES;
        if (c < 7) BAR();
        SBAR();
        LOAD_F(0, sb) MMA_F(1) INTERLEAVE(12)
        SBAR();
      }
      lds_arrive(lds0 + L_CNT + C_DONE * 4, 1, lane);
      } else {
        unsigned go = r16 == 0 ? L_GATE + 16 * g4 : L_ZERO;
        const unsigned gstep = r16 == 0 ? 64 : 0;
        int sb = ST_BYTES;
#pragma unroll
        for (int m = 0; m < 4; ++m) ah[0][m] = f32x4{};
#pragma unroll 1
        for (int c = 0; c < 8; ++c) {
#pragma unroll
          for (int b = 0; b < 2; ++b) {
            const bf16x8 fg = ld128((lds_cptr)L + go); go += gstep;
#pragma unroll
            for (int m = 0; m < 4; ++m) ah[0][m] = MFMA16(fg, ld128((lds_cptr)L + sb + (b ? xo1 : xo0) + m * 2048), ah[0][m]);
          }
          sb = sb == 2 * ST_BYTES ? 0 : sb + ST_BYTES;
          if (c < 7) BAR();
        }
        lds_arrive(lds0 + L_CNT + C_DONE * 4, 1, lane);
        if (h == 7 && lane < 16) {
#pragma unroll
          for (int m = 0; m < 4; ++m)
            __hip_atomic_store((GAS float*)(ws + WS_G) + qb * 256 + v * 64 + m * 16 + lane, gate_from_logit(ah[0][m][0]), RLX_AGENT);
        }
      }
    }
    __builtin_amdgcn_s_setprio(3);
    const u64 tag = make_tag(*(const LAS u64*)(L + L_E));
    const char* kvb = ws + WS_KV + (size_t)(h * 64 + 2 * v) * 2048;
    const unsigned fkv = WS_FKV + (h * 4 + v) * 1024, l16 = lane * 16;
#define KV_DMA(s_) { _Pragma("unroll") for (int i = 0; i < 4; ++i)                                               \
      glds16_sc1(kvb + (qb ^ (s_)) * 16384 + i * 1024, l16, lds0 + (s_) * 16384 + v * 4096 + i * 1024); }
#define KV_READY(s_, n_) { if (lane < (n_)) asm volatile("ds_add_u32 %0, %1" :: "v"(lds0 + L_CNT + (C_READY + (s_)) * 4 + lane * 4), "v"(2u) : "memory"); }
    f32x16 acc[4];
    {
      u32x4 hp[8];
#pragma unroll
      for (int mt = 0; mt < 2; ++mt)
#pragma unroll
        for (int i = 0; i < 4; ++i) {
          f32x4 a = ah[i][2 * mt], b = ah[i][2 * mt + 1];
#pragma unroll
          for (int r = 0; r < 4; ++r) { a[r] = relu(a[r]); b[r] = relu(b[r]); }
          a *= a; b *= b;
          hp[mt * 4 + i] = merge16(a, b, 1.0f); PIN(hp[mt * 4 + i]);
        }
      lds_wait(cnt + C_DONE * 4, 8);
      flags_wait(ws, fkv + (qb ^ 1) * 128, tag);
      KV_DMA(1)
      flags_wait(ws, fkv + (qb ^ (2 + (lane & 1))) * 128, tag);
      KV_READY(1, 1)
      KV_DMA(2) KV_DMA(3)
      const u64 f2 = __hip_atomic_load((gu64*)(ws + fkv + (lane & 7) * 128), RLX_AGENT);
      SBAR();
#pragma unroll
      for (int i = 0; i < 8; ++i)
        st_sc1(hp[i], rs, (ffn ? WS_A + ((qb * 8 + v * 2 + i / 4) * KSTEPS + 32 + hf * 4) * 1024 : OOB) + l16 + (i & 3) * 1024);
      SBAR();
      asm volatile("s_waitcnt vmcnt(8)" ::: "memory");
      KV_READY(2, 2)
      flag_set(ws, WS_FH + ((qb * 4 + v) * 32 + h) * 128, tag, lane);
      if (!__all(f2 == tag)) flags_wait(ws, fkv + (lane & 7) * 128, tag);
      asm volatile("" ::: "memory");
      KV_DMA(4) KV_DMA(5) KV_DMA(6) KV_DMA(7)
      const unsigned short bb[2] = {*(const unsigned short*)(b2 + (ct * 64 + r32) * 2), *(const unsigned short*)(b2 + (ct * 64 + 32 + r32) * 2)};
      const unsigned wt = *(const unsigned*)(wo + (size_t)(v * 128 + (qb & 1) * 64 + lane) * 1024 + ct * 128);
#pragma unroll
      for (int i = 0; i < 2; ++i) st_sc1(u32x4{~0u, ~0u, ~0u, ~0u}, rs, WS_A + ((qb * 8 + v * 2 + i) * KSTEPS + h) * 1024 + l16);
      SBAR();
      asm volatile("s_waitcnt vmcnt(12)" ::: "memory");
      KV_READY(4, 1)
      flag_set(ws, WS_FO + ((qb * 4 + v) * 32 + h) * 128, tag, lane);
      asm volatile("" :: "v"(wt));
      KV_READY(5, 2)
      {
        const bf16x8 e1 = __builtin_bit_cast(bf16x8, u32x4{hh ? 0u : 0x3f80u, 0u, 0u, 0u});
#pragma unroll
        for (int c2 = 0; c2 < 2; ++c2) {
          bf16x8 bf = __builtin_bit_cast(bf16x8, u32x4{(v == 0 && !hh) ? (unsigned)bb[c2] : 0u, 0u, 0u, 0u});
          acc[2 * c2] = MFMA(bf, e1, f32x16{}); PIN(bf);
          acc[2 * c2 + 1] = MFMA(bf, e1, f32x16{});
        }
      }
      {
        LANE_VARS
        const unsigned ra_ = lds0 + L_SCR + v * 6144 + (8 * hh + q4) * 128 + g1 * 32 + p4 * 8 + (q4 >> 1) * 64;
        const char* a0 = ws + WS_A + (size_t)((qb * 8 + u * 2) * KSTEPS + 32 + v * 28) * 1024;
        asm volatile(FFN_ASM
          : "+{v[0:15]}"(acc[0]), "+{v[16:31]}"(acc[1]), "+{v[32:47]}"(acc[2]), "+{v[48:63]}"(acc[3])
          : [wv] "v"((lane >> 3) * 1024 + (((lane & 7) ^ (4 * g1)) << 4)), [l16] "v"(lane * 16), [ra] "v"(ra_), [rb] "v"(ra_ ^ 64),
            [fv] "v"(WS_FH + ((lane & 7) == 7 ? (qb * 4 + v) * 32 + 7 : (qb * 4 + u) * 32 + 8 * v + (lane & 7)) * 128),
            [fo] "v"(WS_FO + ((qb * 4 + u) * 32 + 8 * v + (lane & 7)) * 128), [gv] "v"(WS_G + (qb * 256 + v * 64 + lane) * 4),
            [gl] "v"(lds0 + L_G + (v * 64 + lane) * 4), [cn] "v"(lds0 + L_CNT),
            [wb] "s"(w2 + (size_t)v * 448 * 1024 + ct * 128), [a0] "s"(a0), [a1] "s"(a0 + KSTEPS * 1024),
            [ws] "s"(ws), [tag] "s"(tag), [wo] "s"(wo + (size_t)v * 128 * 1024 + ct * 128), [wl] "s"(lds0 + v * 16384),
            [sl] "s"(__builtin_amdgcn_readfirstlane(lds0 + L_SCR + v * 6144)), [xl] "s"(__builtin_amdgcn_readfirstlane(lds0 + 65536 + v * 4096))
          : FFN_CLOB, "vcc", "scc", "memory");
      }
    }
    {
      LANE_VARS
#pragma unroll
      for (int a = 0; a < 4; ++a)
#pragma unroll
        for (int rq = 0; rq < 4; ++rq)
          *(LAS f32x4*)(L + 65536 + a * 16384 + v * 4096 + rq * 1024 + lane * 16) = f32x4{acc[a][4 * rq], acc[a][4 * rq + 1], acc[a][4 * rq + 2], acc[a][4 * rq + 3]};
      if (lane < 2) asm volatile("ds_add_u32 %0, %1" :: "v"(lds0 + L_CNT + (C_FIN + lane) * 4), "v"(1u) : "memory");
    }
  }
}

extern "C" void kernel_launch(void* const* d_in, const int* in_sizes, int n_in, void* d_out, int out_size,
                              void* d_ws, size_t ws_size, hipStream_t stream) {
  (void)hipFuncSetAttribute((const void*)fused_k, hipFuncAttributeMaxDynamicSharedMemorySize, LDS_BYTES);
  fused_k<<<256, 768, LDS_BYTES, stream>>>((const char*)d_in[0], (const char*)d_in[1], (const char*)d_in[2],
                                           (const char*)d_in[4], (const char*)d_in[5], (const char*)d_in[6],
                                           (const char*)d_in[7], (const char*)d_in[8], (char*)d_ws, (char*)d_out);
}
```
